```python
import torch

def _softmax(x, axis=-1):
    return torch.softmax(x, dim=axis)
def _gelu(x):
    return torch.nn.functional.gelu(x, approximate="tanh")
def _silu(x):
    return torch.nn.functional.silu(x)
def _sigmoid(x):
    return torch.sigmoid(x)
def _layernorm(x, axis=-1, eps=1e-5):
    m = x.mean(dim=axis, keepdim=True)
    v = x.var(dim=axis, keepdim=True, unbiased=False)
    return (x - m) / torch.sqrt(v + eps)
def _scatter_add(tgt, idx, src, axis=0):
    return tgt.clone().index_add_(axis, idx, src)
def _segment_sum(src, seg, n):
    out = torch.zeros((n,) + src.shape[1:], dtype=src.dtype)
    return out.index_add_(0, seg, src)
def _pad(x, pw):
    flat = tuple(v for pair in reversed(pw) for v in pair)
    return torch.nn.functional.pad(x, flat)

kernel_name = 'parallel_sum__ssm_conv_h3__swiglu_ffn__prompt'

def setup_inputs():

    _w = (lambda a: (lambda t: t.to(torch.bfloat16) if t.is_floating_point() else t)(torch.as_tensor(a)))
    def _A_setup():
        B, S, D = 1, 2048, 1024
        ks = 4
        return {
            "q": _w((lambda *s: torch.randn(*s, dtype=torch.bfloat16) * 0.05)(B, S, D)),
            "k": _w((lambda *s: torch.randn(*s, dtype=torch.bfloat16) * 0.8)(B, S, D)),
            "v": _w((lambda *s: torch.randn(*s, dtype=torch.bfloat16) * 0.8)(B, S, D)),
            "shift_k": _w((lambda *s: torch.randn(*s, dtype=torch.bfloat16) * 0.8)(D, ks) * 0.5),
            "ssm_k": _w((lambda *s: torch.randn(*s, dtype=torch.bfloat16) * 0.8)(D, S) * (1.0 / S ** 0.5)),
            "ssm_D": _w((lambda *s: torch.randn(*s, dtype=torch.bfloat16) * 0.8)(D)),
        }
    _A_in = _A_setup()

    def _B_setup():
        B, S, D, F = 1, 2048, 1024, 3584
        return {
            "x": _w((lambda *s: torch.randn(*s, dtype=torch.bfloat16) * 0.05)(B, S, D)),
            "w_gate": _w((lambda *s: torch.randn(*s, dtype=torch.bfloat16) * 0.05)(D, F)),
            "w_up": _w((lambda *s: torch.randn(*s, dtype=torch.bfloat16) * 0.05)(D, F)),
            "w_down": _w((lambda *s: torch.randn(*s, dtype=torch.bfloat16) * 0.05)(F, D)),
        }
    _B_in = _B_setup()

    return {
        "A_q": _A_in["q"],
        "A_k": _A_in["k"],
        "A_v": _A_in["v"],
        "A_shift_k": _A_in["shift_k"],
        "A_ssm_k": _A_in["ssm_k"],
        "A_ssm_D": _A_in["ssm_D"],
        "B_x": _B_in["x"],
        "B_w_gate": _B_in["w_gate"],
        "B_w_up": _B_in["w_up"],
        "B_w_down": _B_in["w_down"],
    }

def reference(A_q, A_k, A_v, A_shift_k, A_ssm_k, A_ssm_D, B_x, B_w_gate, B_w_up, B_w_down):
    def _A_ref(q, k, v, shift_k, ssm_k, ssm_D):
        B, S, D = q.shape
        ks = shift_k.shape[-1]
        k_pad = _pad(k, ((0, 0), (ks - 1, 0), (0, 0)))
        k_shift = torch.zeros_like(k)
        for t in range(ks):
            k_shift = k_shift + k_pad[:, t:t + S, :] * shift_k[:, ks - 1 - t][None, None, :]
        kv = k_shift * v
        L = 2 * S
        kv_pad = _pad(kv, ((0, 0), (0, S), (0, 0)))
        kf_pad = _pad(ssm_k, ((0, 0), (0, S)))
        kv_f = (lambda x, axes=(-2,-1): torch.fft.rfft2(x.float(), dim=axes))(kv_pad[:, None, :, :], axes=(1, 2))
        kf_f = (lambda x, axes=(-2,-1): torch.fft.rfft2(x.float(), dim=axes))(kf_pad[:, None, :], axes=(1, 2))
        kf_f = (lambda x, *d: x.permute(*d))(kf_f, 1, 2, 0)
        y_f = kv_f * kf_f[None, :, :, :]
        y = (lambda x, s, axes=(-2,-1): torch.fft.irfft2(x, s=s, dim=axes).to(torch.bfloat16))(y_f, (1, L), axes=(1, 2))[:, 0, :S, :]
        y = y + kv * ssm_D[None, None, :]
        return q * y

    def _B_ref(x, w_gate, w_up, w_down):
        g = _silu(x @ w_gate)
        u = x @ w_up
        return (g * u) @ w_down

    x = A_q
    xn = _layernorm(x, axis=-1)
    return x + _A_ref(xn, A_k, A_v, A_shift_k, A_ssm_k, A_ssm_D) + _B_ref(xn, B_w_gate, B_w_up, B_w_down)
```

```cpp
#include <hip/hip_runtime.h>
#include <stdint.h>

typedef unsigned short u16;
typedef short bf16x8 __attribute__((ext_vector_type(8)));
typedef short bf16x4 __attribute__((ext_vector_type(4)));
typedef float f32x4 __attribute__((ext_vector_type(4)));
typedef float f32x2 __attribute__((ext_vector_type(2)));
typedef __bf16 bfx2 __attribute__((ext_vector_type(2)));
typedef unsigned u32x4 __attribute__((ext_vector_type(4)));
typedef unsigned u32x2 __attribute__((ext_vector_type(2)));
#define LAS __attribute__((address_space(3)))
#define GAS __attribute__((address_space(1)))

constexpr int SEQ = 2048, DM = 1024, FF = 3584;
constexpr size_t WS_EPOCH = 0, WS_FH = 2048, WS_FZ = 2048 + 224 * 4, WS_FX = 4096, WS_TMO = 8192, WS_FKV = 16384;
constexpr size_t WS_STATS = 65536, WS_CS = 131072, WS_HK0 = (1u << 20), WS_KVT = (4u << 20), WS_ZT = (8u << 20), WS_H = (12u << 20), WS_EX = (28u << 20);

__device__ __forceinline__ unsigned pk_bf16(float a, float b) { f32x2 v = {a, b}; bfx2 r = __builtin_convertvector(v, bfx2); return __builtin_bit_cast(unsigned, r); }
__device__ __forceinline__ float bf_lo(unsigned w) { return __uint_as_float(w << 16); }
__device__ __forceinline__ float bf_hi(unsigned w) { return __uint_as_float(w & 0xffff0000u); }
__device__ __forceinline__ float rnd_bf(float a) { return bf_lo(pk_bf16(a, a)); }

#define WAIT_V(n) asm volatile("s_waitcnt vmcnt(" #n ")" ::: "memory")
#define WAIT_L(n) asm volatile("s_waitcnt lgkmcnt(" #n ")" ::: "memory")
#define BAR() do { asm volatile("" ::: "memory"); __builtin_amdgcn_s_barrier(); asm volatile("" ::: "memory"); } while (0)
#define SCHED() __builtin_amdgcn_sched_barrier(0)

#define STAMP(i)
#define STAMP2(i)
#define ISTAMP(i)
#define ESTAMP(i)
#define GSTAMP(i)
#define STAMP_ARG
#define STAMP_PASS

#define LDFLAG "sc1"
#define STFLAG "sc1"
__device__ __forceinline__ void glds_asm(unsigned voff, const void* sbase, unsigned ldsaddr) {
    asm volatile("s_mov_b32 m0, %2\n\ts_nop 0\n\tglobal_load_lds_dwordx4 %0, %1" :: "v"(voff), "s"(sbase), "s"(ldsaddr) : "memory");
}
__device__ __forceinline__ void gload4(u32x4& dst, unsigned voff, const void* sbase) {
    asm volatile("global_load_dwordx4 %0, %1, %2" : "=v"(dst) : "v"(voff), "s"(sbase) : "memory");
}
__device__ __forceinline__ void gload2(u32x2& dst, unsigned voff, const void* sbase) {
    asm volatile("global_load_dwordx2 %0, %1, %2" : "=v"(dst) : "v"(voff), "s"(sbase) : "memory");
}
__device__ __forceinline__ void gload_sc1(u32x4& dst, unsigned voff, const void* sbase) {
    asm volatile("global_load_dwordx4 %0, %1, %2 " LDFLAG : "=v"(dst) : "v"(voff), "s"(sbase) : "memory");
}
__device__ __forceinline__ void gload2_sc1(u32x2& dst, unsigned voff, const void* sbase) {
    asm volatile("global_load_dwordx2 %0, %1, %2 sc1" : "=v"(dst) : "v"(voff), "s"(sbase) : "memory");
}
__device__ __forceinline__ void gstore_sc1(unsigned voff, void* sbase, u32x4 v) {
    asm volatile("global_store_dwordx4 %0, %1, %2 " STFLAG "\n\ts_nop 1" :: "v"(voff), "v"(v), "s"(sbase) : "memory");
}
__device__ __forceinline__ void gstore_sc1_free(unsigned voff, void* sbase, u32x4 v) {
    asm volatile("global_store_dwordx4 %0, %1, %2 sc1\n\ts_nop 1" :: "v"(voff), "v"(v), "s"(sbase));
}
__device__ __forceinline__ void gstore2_sc1(unsigned voff, void* sbase, u32x2 v) {
    asm volatile("global_store_dwordx2 %0, %1, %2 sc1" :: "v"(voff), "v"(v), "s"(sbase) : "memory");
}
__device__ __forceinline__ float fma_s(float a, float b, float c) { float r; asm("v_fma_f32 %0, %1, %2, %3" : "=v"(r) : "v"(a), "s"(b), "v"(c)); return r; }
__device__ __forceinline__ float fma_v(float a, float b, float c) { float r; asm("v_fma_f32 %0, %1, %2, %3" : "=v"(r) : "v"(a), "v"(b), "v"(c)); return r; }
__device__ __forceinline__ float sub_v(float a, float b) { float r; asm("v_sub_f32 %0, %1, %2" : "=v"(r) : "v"(a), "v"(b)); return r; }
__device__ __forceinline__ float add_v(float a, float b) { float r; asm("v_add_f32 %0, %1, %2" : "=v"(r) : "v"(a), "v"(b)); return r; }
__device__ __forceinline__ float mul_v(float a, float b) { float r; asm("v_mul_f32 %0, %1, %2" : "=v"(r) : "v"(a), "v"(b)); return r; }

__device__ __forceinline__ bf16x8 tr_frag(LAS const char* p, int off_t1) {
    const bf16x4 a = __builtin_amdgcn_ds_read_tr16_b64_v4i16((LAS bf16x4*)p);
    const bf16x4 b = __builtin_amdgcn_ds_read_tr16_b64_v4i16((LAS bf16x4*)(p + off_t1));
    return __builtin_shufflevector(a, b, 0, 1, 2, 3, 4, 5, 6, 7);
}

__device__ __forceinline__ void flag_set(char* ws, size_t off, unsigned tag) { __hip_atomic_store((GAS unsigned*)(ws + off), tag, __ATOMIC_RELAXED, __HIP_MEMORY_SCOPE_AGENT); }
__device__ __forceinline__ void flag_wait(char* ws, size_t off, bool active, unsigned tag) {
    const unsigned long long t0 = __builtin_amdgcn_s_memrealtime();
    for (;;) {
        const unsigned v = active ? __hip_atomic_load((GAS unsigned*)(ws + off), __ATOMIC_RELAXED, __HIP_MEMORY_SCOPE_AGENT) : tag;
        if (__all(v == tag)) break;
        if (__builtin_amdgcn_s_memrealtime() - t0 > 2000000ull) { __hip_atomic_store((GAS unsigned*)(ws + WS_TMO), tag, __ATOMIC_RELAXED, __HIP_MEMORY_SCOPE_AGENT); break; }
        __builtin_amdgcn_s_sleep(1);
    }
}
__device__ __forceinline__ unsigned granule_wait(char* ws, size_t off, unsigned tag) {
    const unsigned long long t0 = __builtin_amdgcn_s_memrealtime();
    for (;;) {
        const unsigned long long g = __hip_atomic_load((GAS unsigned long long*)(ws + off), __ATOMIC_RELAXED, __HIP_MEMORY_SCOPE_AGENT);
        if ((unsigned)(g >> 32) == tag) return (unsigned)g;
        if (__builtin_amdgcn_s_memrealtime() - t0 > 2000000ull) { __hip_atomic_store((GAS unsigned*)(ws + WS_TMO), tag, __ATOMIC_RELAXED, __HIP_MEMORY_SCOPE_AGENT); return (unsigned)g; }
    }
}

template <int CTRL, int RMASK> __device__ __forceinline__ float dpp_add(float v) {
    return v + __builtin_bit_cast(float, __builtin_amdgcn_update_dpp(0, __builtin_bit_cast(int, v), CTRL, RMASK, 0xf, false));
}
__device__ __forceinline__ float wave_sum(float v) {
    v = dpp_add<0x111, 0xf>(v); v = dpp_add<0x112, 0xf>(v); v = dpp_add<0x114, 0xf>(v); v = dpp_add<0x118, 0xf>(v);
    v = dpp_add<0x142, 0xa>(v); v = dpp_add<0x143, 0xc>(v);
    return __builtin_bit_cast(float, __builtin_amdgcn_readlane(__builtin_bit_cast(int, v), 63));
}

struct Params { const u16 *x, *kk, *vv, *shk, *hk, *dvec, *wg, *wu, *wd; u16* out; char* ws; };

__device__ __forceinline__ int kv_s0(int b, int wid) { return (b >> 3) * 64 + (wid & 1) * 32; }
__device__ __forceinline__ int kv_d0(int b, int wid) { return (b & 7) * 128 + (wid >> 1) * 32; }
__device__ __forceinline__ void kv_finish(const Params& p, LAS char* scratch, int s0, int d0, int lane, u32x2 (&kr)[7], const u32x2 (&vr)[4], u32x4 wa, u32x4 wb) {
    const int dg = lane & 7, sg = lane >> 3;
    const bool top = s0 + sg == 0;
#pragma unroll
    for (int i = 0; i < 3; ++i) { kr[i].x = top ? 0u : kr[i].x; kr[i].y = top ? 0u : kr[i].y; }
#pragma unroll
    for (int jp = 0; jp < 2; ++jp) {
        const u32x4 tw = jp ? wb : wa;
        const f32x2 w0 = {bf_lo(tw.x), bf_lo(tw.z)}, w1 = {bf_hi(tw.x), bf_hi(tw.z)}, w2 = {bf_lo(tw.y), bf_lo(tw.w)}, w3 = {bf_hi(tw.y), bf_hi(tw.w)};
        f32x2 kf[7], o[4];
#pragma unroll
        for (int i = 0; i < 7; ++i) kf[i] = (f32x2){bf_lo(kr[i][jp]), bf_hi(kr[i][jp])};
#pragma unroll
        for (int i = 0; i < 4; ++i) o[i] = (kf[i + 3] * w0 + (kf[i + 2] * w1 + (kf[i + 1] * w2 + kf[i] * w3))) * (f32x2){bf_lo(vr[i][jp]), bf_hi(vr[i][jp])};
        const int slot = 7 - sg;
#pragma unroll
        for (int e = 0; e < 2; ++e)
            *(LAS u32x2*)(scratch + (4 * dg + 2 * jp + e) * 64 + (((slot >> 1) ^ (dg & 3)) * 16) + (slot & 1) * 8) = (u32x2){pk_bf16(o[3][e], o[2][e]), pk_bf16(o[1][e], o[0][e])};
    }
#pragma unroll
    for (int h = 0; h < 2; ++h) {
        const int r = (lane >> 2) + 16 * h, c = lane & 3;
        const u32x4 v = *(const LAS u32x4*)(scratch + r * 64 + ((c ^ ((r >> 2) & 3)) * 16));
        gstore_sc1((unsigned)(((d0 + r) * SEQ + (SEQ - 32 - s0) + c * 8) * 2), p.ws + WS_KVT, v);
    }
}
struct KvRegs { u32x2 kr[7], vr[4]; u32x4 wa, wb; };
__device__ __forceinline__ void kv_load_regs(const Params& p, int s0, int d0, int lane, KvRegs& r) {
    const int dg = lane & 7, sb = s0 + 4 * (lane >> 3);
    const unsigned vk = (unsigned)(sb * DM + d0 + 4 * dg) * 2u;
#pragma unroll
    for (int i = 0; i < 3; ++i) gload2(r.kr[i], sb ? vk - (unsigned)(3 - i) * DM * 2u : vk, p.kk);
#pragma unroll
    for (int i = 0; i < 4; ++i) { gload2(r.kr[3 + i], vk, p.kk + i * DM); gload2(r.vr[i], vk, p.vv + i * DM); }
    gload4(r.wa, (unsigned)dg * 32u, p.shk + d0 * 4); gload4(r.wb, (unsigned)dg * 32u, p.shk + d0 * 4 + 8);
}
template <int N> __device__ __forceinline__ void kv_wait_regs(KvRegs& r) {
    asm volatile("s_waitcnt vmcnt(%13)" : "+v"(r.wa), "+v"(r.wb), "+v"(r.kr[0]), "+v"(r.kr[1]), "+v"(r.kr[2]), "+v"(r.kr[3]), "+v"(r.kr[4]), "+v"(r.kr[5]), "+v"(r.kr[6]),
                 "+v"(r.vr[0]), "+v"(r.vr[1]), "+v"(r.vr[2]), "+v"(r.vr[3]) : "n"(N) : "memory");
}
__device__ __forceinline__ void row_stats(const Params& p, int row, int lane, u32x4 xa, u32x4 xb, unsigned tag) {
    float f[16];
#pragma unroll
    for (int i = 0; i < 4; ++i) { f[2 * i] = bf_lo(xa[i]); f[2 * i + 1] = bf_hi(xa[i]); f[8 + 2 * i] = bf_lo(xb[i]); f[9 + 2 * i] = bf_hi(xb[i]); }
    f32x2 s2 = {f[0], f[1]};
#pragma unroll
    for (int i = 2; i < 16; i += 2) s2 += (f32x2){f[i], f[i + 1]};
    const float mean = wave_sum(s2.x + s2.y) * (1.0f / DM);
    f32x2 q = {0.f, 0.f};
#pragma unroll
    for (int i = 0; i < 16; i += 2) { const f32x2 d = (f32x2){f[i], f[i + 1]} - mean; q += d * d; }
    const float var = wave_sum(q.x + q.y) * (1.0f / DM);
    const float sd = __builtin_sqrtf(rnd_bf(rnd_bf(var) + 1e-5f));
    if (lane == 0) gstore2_sc1((unsigned)row * 8u, p.ws + WS_STATS, (u32x2){pk_bf16(mean, sd), tag});
}
__device__ __forceinline__ void epoch_load(const Params& p, unsigned& ep) { asm volatile("global_load_dword %0, %1, %2 sc1" : "=v"(ep) : "v"(0u), "s"(p.ws + WS_EPOCH) : "memory"); }

constexpr size_t G2_BSTEP = (size_t)64 * DM * 2;
struct G2Pos { int tm, tn, kh; };
__device__ __forceinline__ G2Pos g2_pos(int b) { const int xcd = b & 7, idx = b >> 3; return G2Pos{(xcd >> 1) * 4 + (idx & 3), idx >> 2, xcd & 1}; }
__device__ __forceinline__ unsigned g2_voffB(int wid, int lane) { return (unsigned)((wid * 4 + (lane >> 4)) * DM + (((lane & 15) ^ (((lane >> 4) << 1) | ((wid & 2) << 2))) * 8)) * 2u; }
__device__ __forceinline__ const char* g2_baseB(const Params& p, const G2Pos& g) {
    return (const char*)p.wd + ((size_t)g.kh * (FF / 2) * DM + g.tn * 128) * 2;
}
__device__ __forceinline__ void g2_fill(const char* cB, unsigned voffB, unsigned ldsw, int t) {
    glds_asm(voffB, cB + (size_t)t * G2_BSTEP, ldsw + (t & 7) * 16384); glds_asm(voffB, cB + (size_t)t * G2_BSTEP + G2_BSTEP / 2, ldsw + (t & 7) * 16384 + 8192);
}

__device__ __forceinline__ unsigned gemm1_body(const Params& p, LAS char* lds, int b STAMP_ARG) {
    const int tid = threadIdx.x, wid = __builtin_amdgcn_readfirstlane(tid >> 6), lane = tid & 63, wr = wid >> 2, wc = wid & 3, fr = lane & 15, fq = lane >> 4;
    const int xcd = b & 7, idx = b >> 3;
    const int pm = (xcd & 1) * 4 + (idx & 3), pn = (xcd >> 1) * 7 + (idx >> 2);
    const int rot = (pm >> 2) * 8;
    constexpr int HT = 16384, NT = DM / 64;
    const unsigned lds0 = (unsigned)(size_t)lds;
    const unsigned voffA = (unsigned)((wid * 8 + (lane >> 3)) * DM + (((lane & 7) ^ (lane >> 3)) * 8)) * 2u;
    const unsigned voffB = (unsigned)((wid * 4 + (lane >> 4)) * FF + (((lane & 15) ^ (((lane >> 4) << 1) | ((wid & 2) << 2))) * 8)) * 2u;
    const unsigned ldsw = lds0 + (unsigned)wid * 1024u;
    const char* cA = (const char*)p.x + (size_t)pm * 256 * DM * 2;
    const char* cG = (const char*)p.wg + (size_t)pn * 128 * 2;
    const char* cU = (const char*)p.wu + (size_t)pn * 128 * 2;
#define SA(b, h) (((b) * 2 + (h)) * HT)
#define SB(b, h) ((4 + (b) * 2 + (h)) * HT)
#define STAGE_A(b, h, t) do { const char* _g = cA + (size_t)(h) * 128 * DM * 2 + (size_t)(((t) + rot) & 15) * 128; \
        glds_asm(voffA, _g, ldsw + SA(b, h)); glds_asm(voffA, _g + 64 * DM * 2, ldsw + SA(b, h) + 8192); } while (0)
#define STAGE_B(b, h, t) do { const char* _g = ((h) ? cU : cG) + (size_t)(((t) + rot) & 15) * 64 * FF * 2; \
        glds_asm(voffB, _g, ldsw + SB(b, h)); glds_asm(voffB, _g + 32 * FF * 2, ldsw + SB(b, h) + 8192); } while (0)
    const int aoff0 = (wr * 64 + fr) * 128 + ((fq ^ (fr & 7)) * 16), aoff1 = aoff0 ^ 64;
    const int tq = (lane & 15) >> 2, tp = lane & 3;
    const int boff0 = (8 * fq + tq) * 256 + (((4 * wc + (tp >> 1)) ^ ((tq << 1) | ((fq & 1) << 3))) * 16) + 8 * (tp & 1), boff1 = boff0 ^ 32;
#define LDA(dst, b, h) do { _Pragma("unroll") for (int m = 0; m < 4; ++m) { \
        dst[m][0] = *(const LAS bf16x8*)(lds + SA(b, h) + aoff0 + m * 2048); dst[m][1] = *(const LAS bf16x8*)(lds + SA(b, h) + aoff1 + m * 2048); } } while (0)
#define LDB(dst, b, h) do { _Pragma("unroll") for (int k = 0; k < 2; ++k) { \
        dst[0][k] = tr_frag(lds + SB(b, h) + boff0 + k * 8192, 1024); dst[1][k] = tr_frag(lds + SB(b, h) + boff1 + k * 8192, 1024); } } while (0)
#define MMA(ai, bj, At, Bt) do { __builtin_amdgcn_s_setprio(1); _Pragma("unroll") for (int m = 0; m < 4; ++m) _Pragma("unroll") for (int n = 0; n < 2; ++n) _Pragma("unroll") for (int k = 0; k < 2; ++k) \
        acc[ai][bj][m][n] = __builtin_amdgcn_mfma_f32_16x16x32_bf16(Bt[n][k], At[m][k], acc[ai][bj][m][n], 0, 0, 0); __builtin_amdgcn_s_setprio(0); } while (0)
#define CSUM(Bt) do { _Pragma("unroll") for (int n = 0; n < 2; ++n) _Pragma("unroll") for (int k = 0; k < 2; ++k) cs[n] = __builtin_amdgcn_mfma_f32_16x16x32_bf16(Bt[n][k], ones, cs[n], 0, 0, 0); } while (0)
    unsigned ep; epoch_load(p, ep);
    STAGE_B(0, 0, 0); STAGE_B(0, 1, 0); STAGE_A(0, 0, 0); STAGE_A(0, 1, 0);
    u32x4 xa, xb; KvRegs kv;
    kv_load_regs(p, kv_s0(b, wid), kv_d0(b, wid), lane, kv);
    gload4(xa, (unsigned)lane * 16u, p.x + (size_t)(b * 8 + wid) * DM); gload4(xb, (unsigned)lane * 16u, p.x + (size_t)(b * 8 + wid) * DM + 512);
    f32x4 acc[2][2][4][2];
#pragma unroll
    for (int a = 0; a < 2; ++a)
#pragma unroll
        for (int c = 0; c < 2; ++c) {
#pragma unroll
            for (int m = 0; m < 4; ++m)
#pragma unroll
                for (int n = 0; n < 2; ++n) acc[a][c][m][n] = (f32x4){0.f, 0.f, 0.f, 0.f};
            asm volatile("" : "+v"(acc[a][c][0][0]), "+v"(acc[a][c][0][1]), "+v"(acc[a][c][1][0]), "+v"(acc[a][c][1][1]), "+v"(acc[a][c][2][0]), "+v"(acc[a][c][2][1]), "+v"(acc[a][c][3][0]), "+v"(acc[a][c][3][1]));
        }
    bf16x8 At[4][2], B0[2][2], B1[2][2];
    asm volatile("s_waitcnt vmcnt(0)" : "+v"(ep), "+v"(xa), "+v"(xb) :: "memory");
    STAGE_B(1, 0, 1); STAGE_A(1, 0, 1); STAGE_B(1, 1, 1);
    const unsigned tag = __builtin_amdgcn_readfirstlane(ep) + 1u;
    kv_wait_regs<6>(kv);
    kv_finish(p, lds + 131072 + wid * 2048, kv_s0(b, wid), kv_d0(b, wid), lane, kv.kr, kv.vr, kv.wa, kv.wb);
    row_stats(p, b * 8 + wid, lane, xa, xb, tag);
    BAR();
    if (wr == 1) BAR();
    STAMP(1);
    const bf16x8 ones = {0x3f80, 0x3f80, 0x3f80, 0x3f80, 0x3f80, 0x3f80, 0x3f80, 0x3f80};
#define SP1(b, STG, WV, CS) do { LDB(B0, b, 0); LDB(B1, b, 1); SCHED(); LDA(At, b, 0); STG; WV; WAIT_L(0); BAR(); MMA(0, 0, At, B0); MMA(0, 1, At, B1); CS; BAR(); SCHED(); } while (0)
#define SP2(b, STG, WV) do { LDA(At, b, 1); STG; WV; WAIT_L(0); BAR(); MMA(1, 0, At, B0); MMA(1, 1, At, B1); BAR(); SCHED(); } while (0)
#pragma unroll 1
    for (int t = 0; t < NT - 2; t += 2) {
        const bool mine = (t >> 1) == (pm & 3);
        if (t == 2) ISTAMP(3); if (t == 4) ISTAMP(4); if (t == 6) ISTAMP(5); if (t == 8) ISTAMP(6); if (t == 12) ISTAMP(7);
        f32x4 cs[2] = {{0.f, 0.f, 0.f, 0.f}, {0.f, 0.f, 0.f, 0.f}};
        if (t == 10) {
            const char* cg = p.ws + WS_CS + ((size_t)pn * 8 + wid) * 2048;
            glds_asm((unsigned)lane * 16u, cg, lds0 + 131072 + wid * 2048); glds_asm((unsigned)lane * 16u, cg + 1024, lds0 + 131072 + wid * 2048 + 1024);
            if (wid < 2) glds_asm((unsigned)lane * 16u, p.ws + WS_STATS + (size_t)pm * 2048 + wid * 1024, lds0 + 147456 + wid * 1024);
        }
        SP1(0, STAGE_A(1, 1, t + 1), WAIT_V(8), if (mine) { if (wr) CSUM(B1); else CSUM(B0); });
        SP2(0, { STAGE_B(0, 0, t + 2); STAGE_B(0, 1, t + 2); STAGE_A(0, 0, t + 2); },
            { WAIT_V(8); if (t == 0 && lane == 0) flag_set(p.ws, WS_FKV + (size_t)(b * 8 + wid) * 4, tag); });
        SP1(1, STAGE_A(0, 1, t + 2), WAIT_V(8), if (mine) { if (wr) CSUM(B1); else CSUM(B0); });
        SP2(1, { STAGE_B(1, 0, t + 3); STAGE_B(1, 1, t + 3); STAGE_A(1, 0, t + 3); }, WAIT_V(8));
        if (mine) {
            const f32x4 c4 = (fr & 4) ? cs[1] : cs[0];
            const float c2a = (fr & 1) ? c4[1] : c4[0], c2b = (fr & 1) ? c4[3] : c4[2], cv = (fr & 2) ? c2b : c2a;
            if (fr < 8) gstore2_sc1((unsigned)(wr * 128 + wc * 32 + (fr >> 2) * 16 + 4 * fq + (fr & 3)) * 8u, p.ws + WS_CS + ((size_t)pn * 8 + pm) * 2048, (u32x2){__float_as_uint(cv), tag});
        }
    }
    SP1(0, STAGE_A(1, 1, NT - 1), WAIT_V(8), {});
    const char* wB = g2_baseB(p, g2_pos(b)); const unsigned wv = g2_voffB(wid, lane);
    SP2(0, { g2_fill(wB, wv, ldsw, 0); g2_fill(wB, wv, ldsw, 4); g2_fill(wB, wv, ldsw, 5); }, WAIT_V(8));
    {
        int tz = threadIdx.x; asm volatile("" : "+v"(tz));
        if (tz < 256) {
            float s = 0.f; unsigned bad = 0;
#pragma unroll
            for (int i = 0; i < 8; ++i) { const u32x2 g = *(const LAS u32x2*)(lds + 131072 + i * 2048 + tz * 8); s += __uint_as_float(g.x); bad |= g.y ^ tag; }
            if (bad) {
                s = 0.f;
#pragma unroll 1
                for (int i = 0; i < 8; ++i) s += __uint_as_float(granule_wait(p.ws, WS_CS + ((size_t)pn * 8 + i) * 2048 + tz * 8, tag));
            }
            *(LAS float*)(lds + 149504 + tz * 4) = s;
        } else {
            const u32x2 g = *(const LAS u32x2*)(lds + 147456 + (tz - 256) * 8);
            const unsigned v = g.y == tag ? g.x : granule_wait(p.ws, WS_STATS + ((size_t)pm * 256 + tz - 256) * 8, tag);
            *(LAS f32x2*)(lds + 150528 + (tz - 256) * 8) = (f32x2){bf_lo(v), 1.0f / bf_hi(v)};
        }
    }
    SP1(1, g2_fill(wB, wv, ldsw, 1), WAIT_V(8), {});
    SP2(1, g2_fill(wB, wv, ldsw, 2), {});
    if (wr == 0) BAR();
    g2_fill(wB, wv, ldsw, 3);
    STAMP(2);
    {
        int tz = threadIdx.x; asm volatile("" : "+v"(tz));
        const int l2 = tz & 63, er = l2 & 15, eq = l2 >> 4, cm = ((eq & 1) << 1) | (eq >> 1);
        f32x4 cg[2], cu[2];
#pragma unroll
        for (int n = 0; n < 2; ++n) { cg[n] = *(const LAS f32x4*)(lds + 149504 + (wc * 32 + n * 16 + 4 * eq) * 4); cu[n] = *(const LAS f32x4*)(lds + 149504 + (128 + wc * 32 + n * 16 + 4 * eq) * 4); }
        char* hp = p.ws + WS_H + ((size_t)(pm * 16 + wr * 4) * (FF / 32) + pn * 4 + wc) * 1024;
        const unsigned hv = (unsigned)(cm * 16 + er) * 16u;
        f32x2 st[8];
#pragma unroll
        for (int i = 0; i < 8; ++i) st[i] = *(const LAS f32x2*)(lds + 150528 + ((i >> 2) * 128 + wr * 64 + (i & 3) * 16 + er) * 8);
#pragma unroll
        for (int ai = 0; ai < 2; ++ai)
#pragma unroll
            for (int m = 0; m < 4; ++m) {
                const float mu = st[ai * 4 + m].x, ce = st[ai * 4 + m].y * -1.4426950408889634f, kk = -1.4426950408889634f * __builtin_amdgcn_rcpf(st[ai * 4 + m].y);
                unsigned w[2][2];
#pragma unroll
                for (int n = 0; n < 2; ++n)
#pragma unroll
                    for (int j = 0; j < 4; j += 2) {
                        const f32x2 ag = {acc[ai][0][m][n][j], acc[ai][0][m][n][j + 1]}, au = {acc[ai][1][m][n][j], acc[ai][1][m][n][j + 1]};
                        const f32x2 a = (ag - (f32x2){cg[n][j], cg[n][j + 1]} * mu) * ce, u = au - (f32x2){cu[n][j], cu[n][j + 1]} * mu;
                        const f32x2 e = {__builtin_amdgcn_exp2f(a.x), __builtin_amdgcn_exp2f(a.y)};
                        const f32x2 d = e * kk + kk;
                        const f32x2 o = (a * u) * (f32x2){__builtin_amdgcn_rcpf(d.x), __builtin_amdgcn_rcpf(d.y)};
                        w[n][j >> 1] = pk_bf16(o.x, o.y);
                    }
                const auto r0 = __builtin_amdgcn_permlane16_swap(w[0][0], w[1][0], false, false);
                const auto r1 = __builtin_amdgcn_permlane16_swap(w[0][1], w[1][1], false, false);
                gstore_sc1_free(hv, hp + (size_t)(ai * 8 + m) * (FF / 32) * 1024, (u32x4){r0[0], r1[0], r0[1], r1[1]});
            }
    }
    ESTAMP(5);
    WAIT_V(0); ESTAMP(6); BAR();
    if (tid == 0) flag_set(p.ws, WS_FH + (pm * 28 + pn) * 4, tag);
    return tag;
#undef SA
#undef SB
#undef STAGE_A
#undef STAGE_B
#undef LDA
#undef LDB
#undef MMA
#undef CSUM
#undef SP1
#undef SP2
}

__device__ __forceinline__ unsigned conv_body(const Params& p, LAS char* lds_all, int b STAMP_ARG) {
    const int tid = threadIdx.x, wid = __builtin_amdgcn_readfirstlane(tid >> 6), lane = tid & 63, fr = lane & 15, fq = lane >> 4;
    const int grp = b - 224, ch0 = grp * 32 + 24 + wid;
    LAS char* lu = lds_all + wid * 20480;
    LAS char* lh = lu + 4096;
    const unsigned lua = (unsigned)(size_t)lu;
    const int cpy = (fr + 1) & 3;
    const int alc = 2 * (fr - 15 + 8 * fq - cpy + (cpy ? 4 : 0));
    unsigned mk[4];
#pragma unroll
    for (int i = 0; i < 4; ++i) { const int e = fr - 15 + 8 * fq + 2 * i; mk[i] = (e >= 0 ? 0xffffu : 0u) | (e + 1 >= 0 ? 0xffff0000u : 0u); }
    LAS const char* lhc = lh + cpy * 4096; LAS const char* bb = lu + 16 * (254 - 224 - 2 * fr + fq);
    const unsigned v0 = (unsigned)lane * 16u, v1 = (unsigned)(lane ^ 4) * 16u + 2u, v2 = (unsigned)(lane ^ 8) * 16u + 4u, v3 = (unsigned)(lane ^ 12) * 16u + 6u;
    auto fetch_h = [&](int d) {
        const char* hr = d ? (const char*)(p.hk + (size_t)d * SEQ) : p.ws + WS_HK0;
#pragma unroll
        for (int i = 0; i < 4; ++i) {
            glds_asm(v0, hr + i * 1024, lua + 4096 + i * 1024); glds_asm(v2, hr + i * 1024 - 8, lua + 12288 + i * 1024);
            glds_asm(v1, hr + i * 1024 - 8, lua + 8192 + i * 1024); glds_asm(v3, hr + i * 1024 - 8, lua + 16384 + i * 1024);
        }
    };
    auto fetch_u = [&](int d) {
        const char* ur = p.ws + WS_KVT + (size_t)d * SEQ * 2;
#pragma unroll
        for (int i = 0; i < 4; ++i) glds_asm(v0, ur + i * 1024, lua + i * 1024);
    };
    unsigned ep; epoch_load(p, ep);
    unsigned dall;
    asm volatile("global_load_ushort %0, %1, %2" : "=v"(dall) : "v"((unsigned)(lane & 3) * 16u), "s"(p.dvec + ch0 - 24) : "memory");
    unsigned tag;
    {
        KvRegs kv; u32x4 xa, xb;
        kv_load_regs(p, kv_s0(b, wid), kv_d0(b, wid), lane, kv);
        gload4(xa, (unsigned)lane * 16u, p.x + (size_t)(b * 8 + wid) * DM); gload4(xb, (unsigned)lane * 16u, p.x + (size_t)(b * 8 + wid) * DM + 512);
        kv_wait_regs<0>(kv);
        asm volatile("" : "+v"(ep), "+v"(xa), "+v"(xb), "+v"(dall));
        tag = __builtin_amdgcn_readfirstlane(ep) + 1u;
        kv_finish(p, lu, kv_s0(b, wid), kv_d0(b, wid), lane, kv.kr, kv.vr, kv.wa, kv.wb);
        row_stats(p, b * 8 + wid, lane, xa, xb, tag);
    }
    if (ch0 == 24) {
        u32x4 t[4];
#pragma unroll
        for (int i = 0; i < 4; ++i) t[i] = *(const u32x4*)((const char*)p.hk + i * 1024 + lane * 16);
#pragma unroll
        for (int i = 0; i < 4; ++i) *(u32x4*)(p.ws + WS_HK0 + i * 1024 + lane * 16) = t[i];
    }
    if (wid < 4) { fetch_h(ch0); WAIT_V(16); } else WAIT_V(0);
    if (lane == 0) flag_set(p.ws, WS_FKV + (size_t)(b * 8 + wid) * 4, tag);
    if (wid == 4) {
        flag_wait(p.ws, WS_FKV + (size_t)((8 * (lane >> 1) + (grp >> 2)) * 8 + (grp & 3) * 2 + (lane & 1)) * 4, true, tag);
        __builtin_amdgcn_fence(__ATOMIC_ACQUIRE, "agent");
        WAIT_V(0);
    }
    BAR();
    STAMP(1);
    __builtin_amdgcn_s_setprio(1);
    fetch_u(ch0);
    if (wid >= 4) fetch_h(ch0);
#pragma unroll 1
    for (int c = 0; c < 4; ++c) {
        const int d = ch0 - 8 * c;
        WAIT_V(0);
        f32x4 acc[8];
#pragma unroll
        for (int t = 0; t < 8; ++t) acc[t] = (f32x4){0.f, 0.f, 0.f, 0.f};
        bf16x8 A[2][8], B[2][8];
#define CV_LDB(r, i) B[(r) & 1][i] = *(const volatile LAS bf16x8*)(bb + 64 * (r) + 3584 - 512 * (i))
#define CV_LDA(r, i) do { const bf16x4 lo = *(const volatile LAS bf16x4*)(a0 + 512 * (i)), hi = *(const volatile LAS bf16x4*)(a1 + 512 * (i)); \
            A[(r) & 1][i] = __builtin_shufflevector(lo, hi, 0, 1, 2, 3, 4, 5, 6, 7); } while (0)
#define CV_ITEM(r, k) do { if ((k) == 0) CV_LDA(r, 0); else if ((k) < 8) CV_LDB(r, k); else if ((k) == 8) CV_LDB(r, 0); else CV_LDA(r, (k) - 8); } while (0)
        {
            LAS const char* a0 = lhc + (alc ^ (cpy << 6)); LAS const char* a1 = lhc + ((alc + 8) ^ (cpy << 6));
#pragma unroll
            for (int k = 0; k < 16; ++k) CV_ITEM(0, k);
            u32x4 t = __builtin_bit_cast(u32x4, A[0][0]); t.x &= mk[0]; t.y &= mk[1]; t.z &= mk[2]; t.w &= mk[3]; A[0][0] = __builtin_bit_cast(bf16x8, t);
        }
        __builtin_amdgcn_s_setprio(0);
#pragma unroll
        for (int r = 0; r < 8; ++r) {
            if (r == 1) __builtin_amdgcn_s_setprio(2);
            SCHED();
            LAS const char* a0 = lhc + ((64 * (r + 1) + alc) ^ (cpy << 6)); LAS const char* a1 = lhc + ((64 * (r + 1) + alc + 8) ^ (cpy << 6));
            int cnt = 0;
#pragma unroll
            for (int i = 0; i < 8; ++i) {
#pragma unroll
                for (int mm = 0; mm + i < 8; ++mm) {
                    const int m = (mm + 1 + i == 8) ? 0 : mm + 1;
                    if (i == 0 && m == 0) { if (2 * fr + 1 - fq - 4 * r < 0) B[r & 1][0] = (bf16x8){0, 0, 0, 0, 0, 0, 0, 0}; }
                    acc[i + m] = __builtin_amdgcn_mfma_f32_16x16x32_bf16(A[r & 1][i], B[r & 1][m], acc[i + m], 0, 0, 0);
                    if ((cnt & 1) == 1) { if (r < 7 && cnt < 32) CV_ITEM(r + 1, cnt >> 1); SCHED(); }
                    ++cnt;
                }
            }
        }
#undef CV_LDA
#undef CV_LDB
#undef CV_ITEM
        __builtin_amdgcn_s_setprio(1);
        u32x2 uu[8];
#pragma unroll
        for (int t = 0; t < 8; ++t) uu[t] = *(const volatile LAS u32x2*)(lu + 2 * (2044 - (256 * t + 16 * fr + 4 * fq)));
        asm volatile("s_waitcnt lgkmcnt(0)" : "+v"(uu[0]), "+v"(uu[1]), "+v"(uu[2]), "+v"(uu[3]), "+v"(uu[4]), "+v"(uu[5]), "+v"(uu[6]), "+v"(uu[7]) :: "memory");
        if (c < 3) { fetch_u(d - 8); fetch_h(d - 8); }
        const float dd = __uint_as_float((unsigned)__builtin_amdgcn_readlane((int)dall, 3 - c) << 16);
        char* zp = p.ws + WS_ZT + (size_t)d * SEQ * 2;
        const unsigned zv = (unsigned)(16 * fr + 4 * fq) * 2u;
#pragma unroll
        for (int t = 0; t < 8; ++t) {
            const float z0 = fma_s(bf_hi(uu[t].y), dd, acc[t][0]), z1 = fma_s(bf_lo(uu[t].y), dd, acc[t][1]), z2 = fma_s(bf_hi(uu[t].x), dd, acc[t][2]), z3 = fma_s(bf_lo(uu[t].x), dd, acc[t][3]);
            gstore2_sc1(zv, zp + 512 * t, (u32x2){pk_bf16(z0, z1), pk_bf16(z2, z3)});
        }
    }
    __builtin_amdgcn_s_setprio(0);
    STAMP(2);
    WAIT_V(0); BAR();
    if (tid == 0) flag_set(p.ws, WS_FZ + grp * 4, tag);
    {
        const char* wB = g2_baseB(p, g2_pos(b)); const unsigned wv = g2_voffB(wid, lane);
#pragma unroll
        for (int t = 0; t < 6; ++t) g2_fill(wB, wv, (unsigned)(size_t)lds_all + wid * 1024, t);
    }
    WAIT_V(0);
    return tag;
}

template <int N> __device__ __forceinline__ void wait_v4(u32x4 (&a)[4]) { asm volatile("s_waitcnt vmcnt(%4)" : "+v"(a[0]), "+v"(a[1]), "+v"(a[2]), "+v"(a[3]) : "n"(N) : "memory"); }

__device__ __forceinline__ void gemm2_body(const Params& p, LAS char* lds, int b, unsigned tag STAMP_ARG) {
    int tid = threadIdx.x; asm volatile("" : "+v"(tid));
    const int wid = __builtin_amdgcn_readfirstlane(tid >> 6), lane = tid & 63, wm = wid & 3, kp = wid >> 2, fr = lane & 15, fq = lane >> 4;
    const unsigned lds0 = (unsigned)(size_t)lds;
    const G2Pos g = g2_pos(b);
    const unsigned voffB = g2_voffB(wid, lane);
    const unsigned voffA0 = (unsigned)lane * 16u, voffA1 = voffA0 + (FF / 32) * 1024u;
    const char* cA = p.ws + WS_H + ((size_t)(g.tm * 8 + wm * 2) * (FF / 32) + g.kh * (FF / 64) + kp) * 1024;
    const char* cB = g2_baseB(p, g);
    const unsigned ldsw = lds0 + (unsigned)wid * 1024u;
    const int tq = (lane & 15) >> 2, tp = lane & 3;
    const int r0 = g.tm * 128 + g.kh * 64, c0 = g.tn * 128;
    if (wid == 0) flag_wait(p.ws, WS_FH + (size_t)((g.tm >> 1) * 28 + g.kh * 14 + (lane < 14 ? lane : 0)) * 4, true, tag);
    BAR();
    STAMP2(4);
    u32x4 ar[3][4];
#define G2_LDA(set, P) do { const char* _a = cA + (size_t)(P) * 4096; gload_sc1(ar[set][0], voffA0, _a); gload_sc1(ar[set][1], voffA1, _a); \
        gload_sc1(ar[set][2], voffA0, _a + 2048); gload_sc1(ar[set][3], voffA1, _a + 2048); } while (0)
    G2_LDA(0, 0); G2_LDA(1, 1);
    LAS char* lz = lds + 131072; LAS char* lx = lz + 16384;
    {
        const unsigned xoff = (unsigned)((wid * 4 + (lane >> 4)) * DM + (((lane & 15) ^ ((wid * 4 + (lane >> 4)) & 15)) * 8)) * 2u;
        const char* xg = (const char*)p.x + ((size_t)r0 * DM + c0) * 2;
        glds_asm(xoff, xg, ldsw + 147456); glds_asm(xoff, xg + (size_t)32 * DM * 2, ldsw + 147456 + 8192);
    }
    int boff[4];
#pragma unroll
    for (int n = 0; n < 4; ++n) boff[n] = (32 * kp + 8 * fq + tq) * 256 + (((2 * n + (tp >> 1)) ^ ((tq << 1) | ((fq & 1) << 3))) * 16) + 8 * (tp & 1);
    f32x4 acc[2][8];
#pragma unroll
    for (int m = 0; m < 2; ++m)
#pragma unroll
        for (int n = 0; n < 8; ++n) acc[m][n] = (f32x4){0.f, 0.f, 0.f, 0.f};
    bf16x8 bt[2][8];
    LAS const char* lb[2][8];
#pragma unroll
    for (int n = 0; n < 8; ++n) { lb[0][n] = lds + (boff[n & 3] ^ ((n >> 2) * 128)); lb[1][n] = lb[0][n] + 65536; asm volatile("" : "+v"(lb[0][n]), "+v"(lb[1][n])); }
    if (kp == 1) BAR();
#define G2_RD(P, s, n) bt[s][n] = tr_frag(lb[((P) >> 1) & 1][n] + ((P) & 1) * 32768 + (s) * 16384, 1024)
#define G2_PHASE(set, P, DOB, DOA, W, HOOK) do { \
        const char* _a = cA + (size_t)((P) + 2) * 4096; const char* _b = cB + (size_t)(2 * (P) + 6) * G2_BSTEP; const unsigned _l = ldsw + (((P) + 3) & 3) * 32768; \
        _Pragma("unroll") for (int i = 0; i < 4; ++i) { \
            if (DOA) gload_sc1(ar[((set) + 2) % 3][i], (i & 1) ? voffA1 : voffA0, _a + (i >> 1) * 2048); \
            G2_RD(P, 0, 2 * i); G2_RD(P, 0, 2 * i + 1); \
        } \
        HOOK; \
        wait_v4<W>(ar[set]); WAIT_L(0); BAR(); \
        __builtin_amdgcn_s_setprio(1); \
        _Pragma("unroll") for (int n = 0; n < 8; ++n) { \
            acc[0][n] = __builtin_amdgcn_mfma_f32_16x16x32_bf16(bt[0][n], __builtin_bit_cast(bf16x8, ar[set][0]), acc[0][n], 0, 0, 0); SCHED(); \
            G2_RD(P, 1, n); SCHED(); \
            acc[1][n] = __builtin_amdgcn_mfma_f32_16x16x32_bf16(bt[0][n], __builtin_bit_cast(bf16x8, ar[set][1]), acc[1][n], 0, 0, 0); SCHED(); \
        } \
        _Pragma("unroll") for (int n = 0; n < 8; ++n) { \
            acc[0][n] = __builtin_amdgcn_mfma_f32_16x16x32_bf16(bt[1][n], __builtin_bit_cast(bf16x8, ar[set][2]), acc[0][n], 0, 0, 0); \
            acc[1][n] = __builtin_amdgcn_mfma_f32_16x16x32_bf16(bt[1][n], __builtin_bit_cast(bf16x8, ar[set][3]), acc[1][n], 0, 0, 0); SCHED(); \
            if ((n & 1) == 0 && DOB) { glds_asm(voffB, _b + (n >> 1) * (G2_BSTEP / 2), _l + (n >> 1) * 8192); SCHED(); } \
        } \
        __builtin_amdgcn_s_setprio(0); \
        BAR(); SCHED(); } while (0)
    G2_PHASE(0, 0, 1, 1, 10, {});
    G2_PHASE(1, 1, 1, 1, 12, {});
    G2_PHASE(2, 2, 1, 1, 12, {});
    G2_PHASE(0, 3, 1, 1, 12, {});
    GSTAMP(5);
    G2_PHASE(1, 4, 1, 1, 12, { GSTAMP(6); });
    GSTAMP(7);
    G2_PHASE(2, 5, 1, 1, 12, {});
    G2_PHASE(0, 6, 1, 1, 12, {});
    G2_PHASE(1, 7, 1, 1, 12, {});
    unsigned zf;
    G2_PHASE(2, 8, 1, 1, 13, { asm volatile("global_load_dword %0, %1, %2 sc1" : "=v"(zf) : "v"((unsigned)(lane & 3) * 4u), "s"(p.ws + WS_FZ + g.tn * 16) : "memory"); });
    G2_PHASE(0, 9, 1, 1, 13, {});
    G2_PHASE(1, 10, 1, 1, 12, {});
    u32x4 zr[2];
    G2_PHASE(2, 11, 0, 1, 12, {});
    G2_PHASE(0, 12, 0, 0, 6, {
        asm volatile("" : "+v"(zf));
        if (!__all(zf == tag)) flag_wait(p.ws, WS_FZ + (size_t)(g.tn * 4 + (lane & 3)) * 4, true, tag);
        const unsigned zoff = (unsigned)((wid * 8 + (lane >> 3)) * SEQ + (lane & 7) * 8) * 2u;
        const char* zg = p.ws + WS_ZT + ((size_t)c0 * SEQ + r0) * 2;
        gload_sc1(zr[0], zoff, zg); gload_sc1(zr[1], zoff, zg + (size_t)64 * SEQ * 2); });
    G2_PHASE(1, 13, 0, 0, 0, {
        asm volatile("s_waitcnt vmcnt(0)" : "+v"(zr[0]), "+v"(zr[1]) :: "memory");
        const int zrow = wid * 8 + (lane >> 3);
        LAS char* zw = lz + zrow * 128 + (((lane & 7) ^ (zrow & 6)) * 16);
        *(LAS u32x4*)zw = zr[0]; *(LAS u32x4*)(zw + 8192) = zr[1]; });
#undef G2_PHASE
#undef G2_RD
#undef G2_LDA
    if (kp == 0) BAR();
    STAMP2(5);
    f32x4 sum[8];
    {
        LAS char* ex = lds + (wid ^ 4) * 8192 + lane * 16;
#pragma unroll
        for (int n = 0; n < 8; ++n) {
            f32x4 snd;
#pragma unroll
            for (int j = 0; j < 4; ++j) { const float lo = acc[0][n][j], hi = acc[1][n][j]; snd[j] = kp ? lo : hi; sum[n][j] = kp ? hi : lo; }
            *(LAS f32x4*)(ex + n * 1024) = snd;
        }
        WAIT_L(0); BAR();
#pragma unroll
        for (int n = 0; n < 8; ++n) sum[n] += *(const LAS f32x4*)(lds + wid * 8192 + n * 1024 + lane * 16);
    }
    const int slot = (wm & 1) * 2 + kp;
    if ((wm >> 1) != g.kh) {
        char* xo = p.ws + WS_EX + ((size_t)b * 4 + slot) * 8192;
#pragma unroll
        for (int n = 0; n < 8; ++n) gstore_sc1((unsigned)lane * 16u, xo + n * 1024, __builtin_bit_cast(u32x4, sum[n]));
        WAIT_V(0);
        if (lane == 0) flag_set(p.ws, WS_FX + ((size_t)b * 4 + slot) * 4, tag);
    } else {
        const int xr = slot * 16 + fr;
        const unsigned sv = granule_wait(p.ws, WS_STATS + (size_t)(r0 + xr) * 8, tag);
        const float mu = bf_lo(sv), rs = 1.0f / bf_hi(sv);
#pragma unroll
        for (int n = 0; n < 8; ++n) {
            const int zrow = n * 16 + 4 * fq + tq, zc = 2 * slot + (tp >> 1);
            const u32x2 zw = __builtin_bit_cast(u32x2, __builtin_amdgcn_ds_read_tr16_b64_v4i16((LAS bf16x4*)(lz + zrow * 128 + ((zc ^ (zrow & 6)) * 16) + 8 * (tp & 1))));
            const u32x2 xv = *(const LAS u32x2*)(lx + xr * 256 + (((2 * n + (fq >> 1)) ^ (xr & 15)) * 16) + 8 * (fq & 1));
            const float x0 = bf_lo(xv.x), x1 = bf_hi(xv.x), x2 = bf_lo(xv.y), x3 = bf_hi(xv.y);
            sum[n][0] += x0 + rnd_bf(rnd_bf(x0 - mu) * rs) * bf_lo(zw.x); sum[n][1] += x1 + rnd_bf(rnd_bf(x1 - mu) * rs) * bf_hi(zw.x);
            sum[n][2] += x2 + rnd_bf(rnd_bf(x2 - mu) * rs) * bf_lo(zw.y); sum[n][3] += x3 + rnd_bf(rnd_bf(x3 - mu) * rs) * bf_hi(zw.y);
        }
        flag_wait(p.ws, WS_FX + ((size_t)(b ^ 1) * 4 + slot) * 4, true, tag);
        STAMP2(6);
        u32x4 in[8];
#pragma unroll
        for (int n = 0; n < 8; ++n) gload_sc1(in[n], (unsigned)lane * 16u, p.ws + WS_EX + ((size_t)(b ^ 1) * 4 + slot) * 8192 + n * 1024);
        asm volatile("s_waitcnt vmcnt(0)" : "+v"(in[0]), "+v"(in[1]), "+v"(in[2]), "+v"(in[3]), "+v"(in[4]), "+v"(in[5]), "+v"(in[6]), "+v"(in[7]) :: "memory");
        u16* op = p.out + (size_t)(r0 + xr) * DM + c0 + (((fq & 1) << 1) | (fq >> 1)) * 8;
#pragma unroll
        for (int h = 0; h < 4; ++h) {
            const f32x4 u = sum[2 * h] + __builtin_bit_cast(f32x4, in[2 * h]), v = sum[2 * h + 1] + __builtin_bit_cast(f32x4, in[2 * h + 1]);
            const auto q0 = __builtin_amdgcn_permlane16_swap(pk_bf16(u[0], u[1]), pk_bf16(v[0], v[1]), false, false);
            const auto q1 = __builtin_amdgcn_permlane16_swap(pk_bf16(u[2], u[3]), pk_bf16(v[2], v[3]), false, false);
            *(u32x4*)(op + h * 32) = (u32x4){q0[0], q1[0], q0[1], q1[1]};
        }
    }
}

__global__ __launch_bounds__(512, 2) void fused_kernel(Params p) {
    extern __shared__ __attribute__((aligned(16))) char smem[];
    LAS char* lds = (LAS char*)smem;
    const int b = blockIdx.x;
    STAMP(0);
    const unsigned tag = b < 224 ? gemm1_body(p, lds, b STAMP_PASS) : conv_body(p, lds, b STAMP_PASS);
    STAMP2(3);
    gemm2_body(p, lds, b, tag STAMP_PASS);
    if (b == 0 && threadIdx.x < 64) {
        u32x4 f; gload_sc1(f, threadIdx.x * 16u, p.ws + WS_FH);
        asm volatile("s_waitcnt vmcnt(0)" : "+v"(f) :: "memory");
        if (!__all(f.x == tag && f.y == tag && f.z == tag && f.w == tag)) {
#pragma unroll 1
            for (int i = 0; i < 4; ++i) flag_wait(p.ws, WS_FH + (size_t)(i * 64 + threadIdx.x) * 4, true, tag);
        }
        if (threadIdx.x == 0) flag_set(p.ws, WS_EPOCH, tag);
    }
}

extern "C" void kernel_launch(void* const* d_in, const int* in_sizes, int n_in, void* d_out, int out_size, void* d_ws, size_t ws_size, hipStream_t stream) {
    Params p;
    p.x = (const u16*)d_in[0]; p.kk = (const u16*)d_in[1]; p.vv = (const u16*)d_in[2]; p.shk = (const u16*)d_in[3]; p.hk = (const u16*)d_in[4]; p.dvec = (const u16*)d_in[5];
    p.wg = (const u16*)d_in[7]; p.wu = (const u16*)d_in[8]; p.wd = (const u16*)d_in[9]; p.out = (u16*)d_out; p.ws = (char*)d_ws;
    constexpr int LDS_BYTES = 163840;
    (void)hipFuncSetAttribute((const void*)fused_kernel, hipFuncAttributeMaxDynamicSharedMemorySize, LDS_BYTES);
    fused_kernel<<<256, 512, LDS_BYTES, stream>>>(p);
}
```

```cpp
#include <hip/hip_runtime.h>
#include <stdint.h>

typedef unsigned short u16;
typedef short bf16x8 __attribute__((ext_vector_type(8)));
typedef short bf16x4 __attribute__((ext_vector_type(4)));
typedef float f32x4 __attribute__((ext_vector_type(4)));
typedef float f32x2 __attribute__((ext_vector_type(2)));
typedef __bf16 bfx2 __attribute__((ext_vector_type(2)));
typedef unsigned u32x4 __attribute__((ext_vector_type(4)));
typedef unsigned u32x2 __attribute__((ext_vector_type(2)));
#define LAS __attribute__((address_space(3)))
#define GAS __attribute__((address_space(1)))

#define G2P_MM 1
#define G2P_LD 0
#define ORD 1
constexpr int SEQ = 2048, DM = 1024, FF = 3584;
constexpr size_t WS_EPOCH = 0, WS_FH = 2048, WS_FZ = 2048 + 224 * 4, WS_FX = 4096, WS_TMO = 8192, WS_FKV = 16384;
constexpr size_t WS_STATS = 65536, WS_CS = 131072, WS_HK0 = (1u << 20), WS_KVT = (4u << 20), WS_ZT = (8u << 20), WS_H = (12u << 20), WS_EX = (28u << 20);

__device__ __forceinline__ unsigned pk_bf16(float a, float b) { f32x2 v = {a, b}; bfx2 r = __builtin_convertvector(v, bfx2); return __builtin_bit_cast(unsigned, r); }
__device__ __forceinline__ float bf_lo(unsigned w) { return __uint_as_float(w << 16); }
__device__ __forceinline__ float bf_hi(unsigned w) { return __uint_as_float(w & 0xffff0000u); }
__device__ __forceinline__ float rnd_bf(float a) { return bf_lo(pk_bf16(a, a)); }

#define WAIT_V(n) asm volatile("s_waitcnt vmcnt(" #n ")" ::: "memory")
#define WAIT_L(n) asm volatile("s_waitcnt lgkmcnt(" #n ")" ::: "memory")
#define BAR() do { asm volatile("" ::: "memory"); __builtin_amdgcn_s_barrier(); asm volatile("" ::: "memory"); } while (0)
#define SCHED() __builtin_amdgcn_sched_barrier(0)

#define STAMP(i)
#define STAMP2(i)
#define ISTAMP(i)
#define ESTAMP(i)
#define GSTAMP(i)
#define SSTAMP(i)
#define STAMP3(i)
#define WW(i, W) W
#define PSTAMP(i)
#define STAMP_ARG
#define STAMP_PASS

#define LDFLAG "sc1"
#define STFLAG "sc1"
#define HZ
__device__ __forceinline__ void glds_asm(unsigned voff, const void* sbase, unsigned ldsaddr) {
    asm volatile(HZ "s_mov_b32 m0, %2\n\ts_nop 0\n\tglobal_load_lds_dwordx4 %0, %1" :: "v"(voff), "s"(sbase), "s"(ldsaddr) : "memory");
}
__device__ __forceinline__ void touch_asm(unsigned voff, const void* sbase, unsigned ldsaddr) {
    asm volatile(HZ "s_mov_b32 m0, %2\n\ts_nop 0\n\tglobal_load_lds_dword %0, %1" :: "v"(voff), "s"(sbase), "s"(ldsaddr) : "memory");
}
__device__ __forceinline__ void gload4(u32x4& dst, unsigned voff, const void* sbase) {
    asm volatile(HZ "global_load_dwordx4 %0, %1, %2" : "=v"(dst) : "v"(voff), "s"(sbase) : "memory");
}
__device__ __forceinline__ void gload2(u32x2& dst, unsigned voff, const void* sbase) {
    asm volatile(HZ "global_load_dwordx2 %0, %1, %2" : "=v"(dst) : "v"(voff), "s"(sbase) : "memory");
}
__device__ __forceinline__ void gload_sc1(u32x4& dst, unsigned voff, const void* sbase) {
    asm volatile(HZ "global_load_dwordx4 %0, %1, %2 " LDFLAG : "=v"(dst) : "v"(voff), "s"(sbase) : "memory");
}
__device__ __forceinline__ void gload2_sc1(u32x2& dst, unsigned voff, const void* sbase) {
    asm volatile(HZ "global_load_dwordx2 %0, %1, %2 sc1" : "=v"(dst) : "v"(voff), "s"(sbase) : "memory");
}
__device__ __forceinline__ void gstore_sc1(unsigned voff, void* sbase, u32x4 v) {
    asm volatile(HZ "global_store_dwordx4 %0, %1, %2 " STFLAG "\n\ts_nop 1" :: "v"(voff), "v"(v), "s"(sbase) : "memory");
}
__device__ __forceinline__ void gstore_sc1_free(unsigned voff, void* sbase, u32x4 v) {
    asm volatile(HZ "global_store_dwordx4 %0, %1, %2 sc1\n\ts_nop 1" :: "v"(voff), "v"(v), "s"(sbase));
}
__device__ __forceinline__ void gstore2_sc1(unsigned voff, void* sbase, u32x2 v) {
    asm volatile(HZ "global_store_dwordx2 %0, %1, %2 sc1" :: "v"(voff), "v"(v), "s"(sbase) : "memory");
}
__device__ __forceinline__ float fma_s(float a, float b, float c) { float r; asm("v_fma_f32 %0, %1, %2, %3" : "=v"(r) : "v"(a), "s"(b), "v"(c)); return r; }
__device__ __forceinline__ float fma_v(float a, float b, float c) { float r; asm("v_fma_f32 %0, %1, %2, %3" : "=v"(r) : "v"(a), "v"(b), "v"(c)); return r; }
__device__ __forceinline__ float sub_v(float a, float b) { float r; asm("v_sub_f32 %0, %1, %2" : "=v"(r) : "v"(a), "v"(b)); return r; }
__device__ __forceinline__ float add_v(float a, float b) { float r; asm("v_add_f32 %0, %1, %2" : "=v"(r) : "v"(a), "v"(b)); return r; }
__device__ __forceinline__ float mul_v(float a, float b) { float r; asm("v_mul_f32 %0, %1, %2" : "=v"(r) : "v"(a), "v"(b)); return r; }

__device__ __forceinline__ bf16x8 tr_frag(LAS const char* p, int off_t1) {
    const bf16x4 a = __builtin_amdgcn_ds_read_tr16_b64_v4i16((LAS bf16x4*)p);
    const bf16x4 b = __builtin_amdgcn_ds_read_tr16_b64_v4i16((LAS bf16x4*)(p + off_t1));
    return __builtin_shufflevector(a, b, 0, 1, 2, 3, 4, 5, 6, 7);
}

__device__ __forceinline__ void flag_set(char* ws, size_t off, unsigned tag) { __hip_atomic_store((GAS unsigned*)(ws + off), tag, __ATOMIC_RELAXED, __HIP_MEMORY_SCOPE_AGENT); }
__device__ __forceinline__ void flag_wait(char* ws, size_t off, bool active, unsigned tag) {
    const unsigned long long t0 = __builtin_amdgcn_s_memrealtime();
    for (;;) {
        const unsigned v = active ? __hip_atomic_load((GAS unsigned*)(ws + off), __ATOMIC_RELAXED, __HIP_MEMORY_SCOPE_AGENT) : tag;
        if (__all(v == tag)) break;
        if (__builtin_amdgcn_s_memrealtime() - t0 > 2000000ull) { __hip_atomic_store((GAS unsigned*)(ws + WS_TMO), tag, __ATOMIC_RELAXED, __HIP_MEMORY_SCOPE_AGENT); break; }
        __builtin_amdgcn_s_sleep(1);
    }
}
__device__ __forceinline__ unsigned granule_wait(char* ws, size_t off, unsigned tag) {
    const unsigned long long t0 = __builtin_amdgcn_s_memrealtime();
    for (;;) {
        const unsigned long long g = __hip_atomic_load((GAS unsigned long long*)(ws + off), __ATOMIC_RELAXED, __HIP_MEMORY_SCOPE_AGENT);
        if ((unsigned)(g >> 32) == tag) return (unsigned)g;
        if (__builtin_amdgcn_s_memrealtime() - t0 > 2000000ull) { __hip_atomic_store((GAS unsigned*)(ws + WS_TMO), tag, __ATOMIC_RELAXED, __HIP_MEMORY_SCOPE_AGENT); return (unsigned)g; }
    }
}

template <int CTRL, int RMASK> __device__ __forceinline__ float dpp_add(float v) {
    return v + __builtin_bit_cast(float, __builtin_amdgcn_update_dpp(0, __builtin_bit_cast(int, v), CTRL, RMASK, 0xf, false));
}
__device__ __forceinline__ float wave_sum(float v) {
    v = dpp_add<0x111, 0xf>(v); v = dpp_add<0x112, 0xf>(v); v = dpp_add<0x114, 0xf>(v); v = dpp_add<0x118, 0xf>(v);
    v = dpp_add<0x142, 0xa>(v); v = dpp_add<0x143, 0xc>(v);
    return __builtin_bit_cast(float, __builtin_amdgcn_readlane(__builtin_bit_cast(int, v), 63));
}

struct Params { const u16 *x, *kk, *vv, *shk, *hk, *dvec, *wg, *wu, *wd; u16* out; char* ws; };
typedef const __attribute__((address_space(4))) Params* KP;
__device__ __forceinline__ KP fresh(KP q) { asm volatile("" : "+s"(q)); return q; }
__device__ __forceinline__ int fresh(int v) { asm volatile("" : "+v"(v)); return v; }

struct P0Regs { unsigned ep; u32x4 tw, xa, xb; };
__device__ __forceinline__ void p0_touch(KP p, unsigned junk, int b, int wid, int lane) {
    size_t ak = (size_t)p->kk, av = (size_t)p->vv, ax = (size_t)p->x, as = (size_t)p->shk, aw = (size_t)p->ws;
    asm volatile("" : "+s"(ak), "+s"(av), "+s"(ax), "+s"(as), "+s"(aw));
    const int rb = (b >> 3) * 8 + wid, i = lane < 22 ? lane : lane - 22, r = rb * 8 + (i >> 1) - (lane < 22 ? 3 : 0);
    unsigned o = (unsigned)((r < 0 ? 0 : r) * DM * 2 + (b & 7) * 256 + (i & 1) * 128);
    size_t base = lane < 22 ? ak : av;
    o = lane >= 38 ? (unsigned)((b * 8 + wid) * DM * 2 + (lane - 38) * 128) : o; base = lane >= 38 ? ax : base;
    o = lane >= 54 ? (unsigned)((b & 7) * 1024 + (lane - 54) * 128) : o; base = lane >= 54 ? as : base;
    o = lane >= 62 ? (lane == 62 ? (unsigned)WS_EPOCH : (unsigned)WS_KVT + (unsigned)(255 - rb) * 16384u) : o; base = lane >= 62 ? aw : base;
    const size_t a = base + o;
    asm volatile("s_mov_b32 m0, %1\n\ts_nop 0\n\tglobal_load_lds_dword %0, off" :: "v"(a), "s"(junk) : "memory");
}
__device__ __forceinline__ void p0_stage(KP p, unsigned lk, unsigned lv, int b, int wid, int lane) {
    const int rb = (b >> 3) * 8 + wid;
    const u16* pk = p->kk + (size_t)rb * 8 * DM + (b & 7) * 128; const u16* pv = p->vv + (size_t)rb * 8 * DM + (b & 7) * 128;
    const unsigned vr = (unsigned)((lane >> 4) * DM * 2 + (lane & 15) * 16);
    glds_asm(vr, pk + (rb ? -3 * DM : 0), lk); glds_asm(vr, pk + DM, lk + 1024); glds_asm(vr, pk + 4 * DM, lk + 1792);
    glds_asm(vr, pv, lv); glds_asm(vr, pv + 4 * DM, lv + 1024);
}
__device__ __forceinline__ void p0_load(KP p, P0Regs& r, int b, int wid, int lane) {
    const u16* px = p->x + (size_t)(b * 8 + wid) * DM; const unsigned v16 = (unsigned)lane * 16u;
    gload4(r.tw, v16, p->shk + (b & 7) * 512);
    gload4(r.xa, v16, px); gload4(r.xb, v16, px + 512);
    asm volatile(HZ "global_load_dword %0, %1, %2 sc1" : "=v"(r.ep) : "v"(0u), "s"(p->ws + WS_EPOCH) : "memory");
}
template <int N> __device__ __forceinline__ void p0_wait(P0Regs& r) { asm volatile("s_waitcnt vmcnt(%4)" : "+v"(r.ep), "+v"(r.tw), "+v"(r.xa), "+v"(r.xb) : "n"(N) : "memory"); }
__device__ __forceinline__ void p0_kv(char* ws, const P0Regs& r, LAS const char* lk, LAS const char* lv, int b, int wid, int lane) {
    const f32x2 w0 = {bf_lo(r.tw.x), bf_lo(r.tw.z)}, w1 = {bf_hi(r.tw.x), bf_hi(r.tw.z)}, w2 = {bf_lo(r.tw.y), bf_lo(r.tw.w)}, w3 = {bf_hi(r.tw.y), bf_hi(r.tw.w)};
    const int rb = (b >> 3) * 8 + wid;
    f32x2 kf[11], o[8];
#pragma unroll
    for (int i = 0; i < 11; ++i) {
        unsigned w = *(const LAS unsigned*)(lk + i * 256 + lane * 4);
        if (i < 4 && rb == 0) w = i == 3 ? *(const LAS unsigned*)(lk + lane * 4) : 0u;
        kf[i] = (f32x2){bf_lo(w), bf_hi(w)};
    }
#pragma unroll
    for (int i = 0; i < 8; ++i) { const unsigned v = *(const LAS unsigned*)(lv + i * 256 + lane * 4); o[i] = (kf[i + 3] * w0 + (kf[i + 2] * w1 + (kf[i + 1] * w2 + kf[i] * w3))) * (f32x2){bf_lo(v), bf_hi(v)}; }
#pragma unroll
    for (int e = 0; e < 2; ++e)
        gstore_sc1((unsigned)lane * 32u, ws + WS_KVT + (size_t)(255 - rb) * 16384 + (b & 7) * 2048 + e * 16,
                   (u32x4){pk_bf16(o[7][e], o[6][e]), pk_bf16(o[5][e], o[4][e]), pk_bf16(o[3][e], o[2][e]), pk_bf16(o[1][e], o[0][e])});
}
__device__ __forceinline__ void flag_set_all(char* ws, size_t off, unsigned tag) {
    asm volatile(HZ "global_store_dword %0, %1, %2 sc1" :: "v"(0u), "v"(tag), "s"(ws + off) : "memory");
}
__device__ __forceinline__ void row_stats(char* ws, int row, int lane, u32x4 xa, u32x4 xb, unsigned tag) {
    float f[16];
#pragma unroll
    for (int i = 0; i < 4; ++i) { f[2 * i] = bf_lo(xa[i]); f[2 * i + 1] = bf_hi(xa[i]); f[8 + 2 * i] = bf_lo(xb[i]); f[9 + 2 * i] = bf_hi(xb[i]); }
    f32x2 s2 = {f[0], f[1]};
#pragma unroll
    for (int i = 2; i < 16; i += 2) s2 += (f32x2){f[i], f[i + 1]};
    const float mean = wave_sum(s2.x + s2.y) * (1.0f / DM);
    f32x2 q = {0.f, 0.f};
#pragma unroll
    for (int i = 0; i < 16; i += 2) { const f32x2 d = (f32x2){f[i], f[i + 1]} - mean; q += d * d; }
    const float var = wave_sum(q.x + q.y) * (1.0f / DM);
    const float sd = __builtin_sqrtf(rnd_bf(rnd_bf(var) + 1e-5f));
    if (lane == 0) gstore2_sc1((unsigned)row * 8u, ws + WS_STATS, (u32x2){pk_bf16(mean, sd), tag});
}

constexpr size_t G2_BSTEP = (size_t)64 * DM * 2;
struct G2Pos { int tm, tn, kh; };
__device__ __forceinline__ G2Pos g2_pos(int b) { const int xcd = b & 7, idx = b >> 3; return G2Pos{(xcd >> 1) * 4 + (idx & 3), idx >> 2, xcd & 1}; }
__device__ __forceinline__ unsigned g2_voffB(int wid, int lane) { return (unsigned)((wid * 4 + (lane >> 4)) * DM + (((lane & 15) ^ (((lane >> 4) << 1) | ((wid & 2) << 2))) * 8)) * 2u; }
__device__ __forceinline__ const char* g2_baseB(KP p, const G2Pos& g) {
    return (const char*)p->wd + ((size_t)g.kh * (FF / 2) * DM + g.tn * 128) * 2;
}
__device__ __forceinline__ void g2_fill(const char* cB, unsigned voffB, unsigned ldsw, int t) {
    glds_asm(voffB, cB + (size_t)t * G2_BSTEP, ldsw + (t & 7) * 16384); glds_asm(voffB, cB + (size_t)t * G2_BSTEP + G2_BSTEP / 2, ldsw + (t & 7) * 16384 + 8192);
}

__device__ __forceinline__ unsigned gemm1_body(KP p, LAS char* lds, int b STAMP_ARG) {
    const int tid = threadIdx.x, wid = __builtin_amdgcn_readfirstlane(tid >> 6), lane = tid & 63, wr = wid >> 2, wc = wid & 3, fr = lane & 15, fq = lane >> 4;
    const int xcd = b & 7, idx = b >> 3;
    const int pm = (xcd & 1) * 4 + (idx & 3), pn = (xcd >> 1) * 7 + (idx >> 2);
    const int rot = (pm >> 2) * 8;
    constexpr int HT = 16384, NT = DM / 64;
    const unsigned lds0 = (unsigned)(size_t)lds;
    const unsigned voffA = (unsigned)((wid * 8 + (lane >> 3)) * DM + (((lane & 7) ^ (lane >> 3)) * 8)) * 2u;
    const unsigned voffB = (unsigned)((wid * 4 + (lane >> 4)) * FF + (((lane & 15) ^ (((lane >> 4) << 1) | ((wid & 2) << 2))) * 8)) * 2u;
    const unsigned ldsw = lds0 + (unsigned)wid * 1024u;
    const char* cA = (const char*)p->x + (size_t)pm * 256 * DM * 2;
    const char* cG = (const char*)p->wg + (size_t)pn * 128 * 2;
    const char* cU = (const char*)p->wu + (size_t)pn * 128 * 2;
#define SA(b, h) (((b) * 2 + (h)) * HT)
#define SB(b, h) ((4 + (b) * 2 + (h)) * HT)
#define STAGE_A(b, h, t) do { const char* _g = cA + (size_t)(h) * 128 * DM * 2 + (size_t)(((t) + rot) & 15) * 128; \
        glds_asm(voffA, _g, ldsw + SA(b, h)); glds_asm(voffA, _g + 64 * DM * 2, ldsw + SA(b, h) + 8192); } while (0)
#define STAGE_B(b, h, t) do { const char* _g = ((h) ? cU : cG) + (size_t)(((t) + rot) & 15) * 64 * FF * 2; \
        glds_asm(voffB, _g, ldsw + SB(b, h)); glds_asm(voffB, _g + 32 * FF * 2, ldsw + SB(b, h) + 8192); } while (0)
    const int aoff0 = (wr * 64 + fr) * 128 + ((fq ^ (fr & 7)) * 16), aoff1 = aoff0 ^ 64;
    const int tq = (lane & 15) >> 2, tp = lane & 3;
    const int boff0 = (8 * fq + tq) * 256 + (((4 * wc + (tp >> 1)) ^ ((tq << 1) | ((fq & 1) << 3))) * 16) + 8 * (tp & 1), boff1 = boff0 ^ 32;
#define LDA(dst, b, h) do { _Pragma("unroll") for (int m = 0; m < 4; ++m) { \
        dst[m][0] = *(const LAS bf16x8*)(la0 + (SA(b, h) + m * 2048)); dst[m][1] = *(const LAS bf16x8*)(la1 + (SA(b, h) + m * 2048)); } } while (0)
    LAS const char* lb0 = lds + 65536 + boff0; LAS const char* lb1 = lds + 65536 + boff1; LAS const char* la0 = lds + aoff0; LAS const char* la1 = lds + aoff1;
    asm volatile("" : "+v"(lb0), "+v"(lb1), "+v"(la0), "+v"(la1));
#define LDB(dst, b, h) do { _Pragma("unroll") for (int k = 0; k < 2; ++k) { \
        dst[0][k] = tr_frag(lb0 + (SB(b, h) - 65536 + k * 8192), 1024); dst[1][k] = tr_frag(lb1 + (SB(b, h) - 65536 + k * 8192), 1024); } } while (0)
#define MMA(ai, bj, At, Bt) do { __builtin_amdgcn_s_setprio(0); _Pragma("unroll") for (int m = 0; m < 4; ++m) _Pragma("unroll") for (int n = 0; n < 2; ++n) _Pragma("unroll") for (int k = 0; k < 2; ++k) \
        acc[ai][bj][m][n] = __builtin_amdgcn_mfma_f32_16x16x32_bf16(Bt[n][k], At[m][k], acc[ai][bj][m][n], 0, 0, 0); __builtin_amdgcn_s_setprio(2); } while (0)
#define CSUM(Bt) do { u32x4 o4 = {0x3f803f80u, 0x3f803f80u, 0x3f803f80u, 0x3f803f80u}; asm volatile("" : "+v"(o4)); const bf16x8 ones = __builtin_bit_cast(bf16x8, o4); _Pragma("unroll") for (int n = 0; n < 2; ++n) _Pragma("unroll") for (int k = 0; k < 2; ++k) cs[n] = __builtin_amdgcn_mfma_f32_16x16x32_bf16(Bt[n][k], ones, cs[n], 0, 0, 0); } while (0)
    char* ws = p->ws;
    const int tslot = (wid & 1) + (wid >> 2) * 2;
    const unsigned voffT = (wid & 2) ? (unsigned)((((lane & 15) >> 1) + ((lane & 16) && idx < 4 ? 224 : 0)) * DM * 2 + (lane & 1) * 128) : (unsigned)(((lane & 31) >> 1) * FF * 2 + (lane & 1) * 128);
    const char* tb = (wid & 2) ? cA + (size_t)((idx >> 2) * 32 + tslot * 8) * DM * 2 : ((idx & 2) ? cU : cG) + (size_t)((idx & 1) * 64 + tslot * 16) * FF * 2;
    const unsigned tstep = (wid & 2) ? 128u : 64u * FF * 2u;
#define TOUCH(kt) touch_asm(voffT, tb + (size_t)(((kt) + rot) & 15) * tstep, lds0 + 163584)
    PSTAMP(3);
    P0Regs pr; p0_load(p, pr, b, wid, lane);
    STAGE_B(0, 0, 0); STAGE_B(0, 1, 0); STAGE_A(0, 0, 0); STAGE_A(0, 1, 0);
    PSTAMP(4);
    f32x4 acc[2][2][4][2];
#pragma unroll
    for (int a = 0; a < 2; ++a)
#pragma unroll
        for (int c = 0; c < 2; ++c) {
#pragma unroll
            for (int m = 0; m < 4; ++m)
#pragma unroll
                for (int n = 0; n < 2; ++n) acc[a][c][m][n] = (f32x4){0.f, 0.f, 0.f, 0.f};
            asm volatile("" : "+v"(acc[a][c][0][0]), "+v"(acc[a][c][0][1]), "+v"(acc[a][c][1][0]), "+v"(acc[a][c][1][1]), "+v"(acc[a][c][2][0]), "+v"(acc[a][c][2][1]), "+v"(acc[a][c][3][0]), "+v"(acc[a][c][3][1]));
        }
    bf16x8 At[4][2], B0[2][2], B1[2][2];
    p0_wait<8>(pr);
    PSTAMP(5);
    STAGE_B(1, 0, 1); STAGE_A(1, 0, 1); STAGE_B(1, 1, 1);
    PSTAMP(6);
    const unsigned tag = __builtin_amdgcn_readfirstlane(pr.ep) + 1u;
    p0_kv(ws, pr, lds + 131072 + wid * 3072, lds + 49152 + wid * 2048, b, wid, lane); row_stats(ws, b * 8 + wid, lane, pr.xa, pr.xb, tag);
    PSTAMP(7);
    WAIT_V(9);
    BAR();
    __builtin_amdgcn_s_setprio(2);
    if (wr == 1) BAR();
    STAMP3(1);
#define SP1(b, STG, WV, CS) do { LDB(B0, b, 0); LDB(B1, b, 1); SCHED(); LDA(At, b, 0); STG; WV; WAIT_L(0); BAR(); MMA(0, 0, At, B0); MMA(0, 1, At, B1); CS; BAR(); SCHED(); } while (0)
#define SP2(b, STG, WV) do { LDA(At, b, 1); STG; WV; WAIT_L(0); BAR(); MMA(1, 0, At, B0); MMA(1, 1, At, B1); BAR(); SCHED(); } while (0)
    f32x4 cs[2] = {{0.f, 0.f, 0.f, 0.f}, {0.f, 0.f, 0.f, 0.f}};
#pragma unroll 1
    for (int t = 0; t < 8; t += 2) {
        const bool mine = (t >> 1) == (pm & 3);
        if (t == 2) ISTAMP(3); if (t == 4) ISTAMP(4); if (t == 6) ISTAMP(5);
        SP1(0, STAGE_A(1, 1, t + 1), { if (t) WAIT_V(9); else TOUCH(4); }, if (mine) { if (wr) CSUM(B1); else CSUM(B0); });
        SP2(0, { STAGE_B(0, 0, t + 2); STAGE_B(0, 1, t + 2); STAGE_A(0, 0, t + 2); }, { if (t) WAIT_V(9); else WAIT_V(12); });
        SP1(1, STAGE_A(0, 1, t + 2), { WAIT_V(8); if (t == 0) flag_set_all(ws, WS_FKV + (size_t)((b & 7) * 256 + (b >> 3) * 8 + wid) * 4, tag); }, if (mine) { if (wr) CSUM(B1); else CSUM(B0); });
        SP2(1, { STAGE_B(1, 0, t + 3); STAGE_B(1, 1, t + 3); STAGE_A(1, 0, t + 3); }, { WAIT_V(8); TOUCH(t + 6); });
    }
    ISTAMP(6);
#pragma unroll 1
    for (int t = 8; t < NT - 2; t += 2) {
        if (t == 8) {
            const int ln = fresh(lane), er = ln & 7;
            const f32x4 c4 = (er & 4) ? cs[1] : cs[0];
            const float c2a = (er & 1) ? c4[1] : c4[0], c2b = (er & 1) ? c4[3] : c4[2], cv = (er & 2) ? c2b : c2a;
            gstore2_sc1((unsigned)(wr * 128 + wc * 32 + (er >> 2) * 16 + 4 * (ln >> 4) + (er & 3)) * 8u, ws + WS_CS + ((size_t)pn * 8 + pm) * 2048, (u32x2){__float_as_uint(cv), tag});
        }
        if (t == 12) {
            ISTAMP(7);

            const char* w = fresh(p)->ws; const char* cg = w + WS_CS + ((size_t)pn * 8 + wid) * 2048; const unsigned v = (unsigned)fresh(lane) * 16u;
            glds_asm(v, cg, lds0 + 131072 + wid * 2048); glds_asm(v, cg + 1024, lds0 + 131072 + wid * 2048 + 1024);
            if (wid < 2) glds_asm(v, w + WS_STATS + (size_t)pm * 2048 + wid * 1024, lds0 + 147456 + wid * 1024);

        }
        SP1(0, STAGE_A(1, 1, t + 1), WAIT_V(9), {});
        SP2(0, { STAGE_B(0, 0, t + 2); STAGE_B(0, 1, t + 2); STAGE_A(0, 0, t + 2); }, WAIT_V(9));
        SP1(1, STAGE_A(0, 1, t + 2), WAIT_V(8), {});
        SP2(1, { STAGE_B(1, 0, t + 3); STAGE_B(1, 1, t + 3); STAGE_A(1, 0, t + 3); },
            { WAIT_V(8);
              if (t == 8) TOUCH(14);
              else touch_asm((unsigned)(fresh(lane) & 15) * 128u, (const char*)fresh(p)->wd + (size_t)((b * 8 + wid) * 2 + ((t - 10) >> 1)) * 2048, lds0 + 163584); });
    }
    SP1(0, STAGE_A(1, 1, NT - 1), WAIT_V(8), {});

    p = fresh(p);
    const char* wB = g2_baseB(p, g2_pos(b)); unsigned wv = g2_voffB(wid, fresh(lane)); asm volatile("" : "+v"(wv));

    SP2(0, { g2_fill(wB, wv, ldsw, 0); g2_fill(wB, wv, ldsw, 4); g2_fill(wB, wv, ldsw, 5); }, WAIT_V(8));

    {
        int tz = threadIdx.x; asm volatile("" : "+v"(tz));
        if (tz < 256) {
            float s = 0.f; unsigned bad = 0;
#pragma unroll
            for (int i = 0; i < 8; ++i) { const u32x2 g = *(const LAS u32x2*)(lds + 131072 + i * 2048 + tz * 8); s += __uint_as_float(g.x); bad |= g.y ^ tag; }
            if (bad) {
                s = 0.f;
#pragma unroll 1
                for (int i = 0; i < 8; ++i) s += __uint_as_float(granule_wait(p->ws, WS_CS + ((size_t)pn * 8 + i) * 2048 + tz * 8, tag));
            }
            *(LAS float*)(lds + 149504 + tz * 4) = s;
        } else {
            const u32x2 g = *(const LAS u32x2*)(lds + 147456 + (tz - 256) * 8);
            const unsigned v = g.y == tag ? g.x : granule_wait(p->ws, WS_STATS + ((size_t)pm * 256 + tz - 256) * 8, tag);
            *(LAS f32x2*)(lds + 150528 + (tz - 256) * 8) = (f32x2){bf_lo(v), 1.0f / bf_hi(v)};
        }
    }

    SP1(1, g2_fill(wB, wv, ldsw, 1), WAIT_V(8), {});
    SP2(1, g2_fill(wB, wv, ldsw, 2), {});
    if (wr == 0) BAR();
    g2_fill(wB, wv, ldsw, 3);
    STAMP3(2);
    {
        int tz = threadIdx.x; asm volatile("" : "+v"(tz));
        const int l2 = tz & 63, er = l2 & 15, eq = l2 >> 4, cm = ((eq & 1) << 1) | (eq >> 1);
        f32x4 cg[2], cu[2];
#pragma unroll
        for (int n = 0; n < 2; ++n) { cg[n] = *(const LAS f32x4*)(lds + 149504 + (wc * 32 + n * 16 + 4 * eq) * 4); cu[n] = *(const LAS f32x4*)(lds + 149504 + (128 + wc * 32 + n * 16 + 4 * eq) * 4); }
        char* hp = p->ws + WS_H + ((size_t)(pm * 16 + wr * 4) * (FF / 32) + pn * 4 + wc) * 1024;
        const unsigned hv = (unsigned)(cm * 16 + er) * 16u;
        f32x2 st[8];
#pragma unroll
        for (int i = 0; i < 8; ++i) st[i] = *(const LAS f32x2*)(lds + 150528 + ((i >> 2) * 128 + wr * 64 + (i & 3) * 16 + er) * 8);
#pragma unroll
        for (int ai = 0; ai < 2; ++ai)
#pragma unroll
            for (int m = 0; m < 4; ++m) {
                const float mu = st[ai * 4 + m].x, ce = st[ai * 4 + m].y * -1.4426950408889634f, kk = -1.4426950408889634f * __builtin_amdgcn_rcpf(st[ai * 4 + m].y);
                unsigned w[2][2];
#pragma unroll
                for (int n = 0; n < 2; ++n)
#pragma unroll
                    for (int j = 0; j < 4; j += 2) {
                        const f32x2 ag = {acc[ai][0][m][n][j], acc[ai][0][m][n][j + 1]}, au = {acc[ai][1][m][n][j], acc[ai][1][m][n][j + 1]};
                        const f32x2 a = (ag - (f32x2){cg[n][j], cg[n][j + 1]} * mu) * ce, u = au - (f32x2){cu[n][j], cu[n][j + 1]} * mu;
                        const f32x2 e = {__builtin_amdgcn_exp2f(a.x), __builtin_amdgcn_exp2f(a.y)};
                        const f32x2 d = e * kk + kk;
                        const f32x2 o = (a * u) * (f32x2){__builtin_amdgcn_rcpf(d.x), __builtin_amdgcn_rcpf(d.y)};
                        w[n][j >> 1] = pk_bf16(o.x, o.y);
                    }
                const auto r0 = __builtin_amdgcn_permlane16_swap(w[0][0], w[1][0], false, false);
                const auto r1 = __builtin_amdgcn_permlane16_swap(w[0][1], w[1][1], false, false);
                gstore_sc1_free(hv, hp + (size_t)(ai * 8 + m) * (FF / 32) * 1024, (u32x4){r0[0], r1[0], r0[1], r1[1]});
            }
    }
    ESTAMP(5);
    WAIT_V(0); ESTAMP(6); BAR();
    if (tid == 0) flag_set(p->ws, WS_FH + (pm * 28 + pn) * 4, tag);
    return tag;
#undef SA
#undef SB
#undef STAGE_A
#undef STAGE_B
#undef LDA
#undef LDB
#undef MMA
#undef CSUM
#undef TOUCH
#undef SP1
#undef SP2
}

__device__ __forceinline__ unsigned conv_body(KP p, LAS char* lds_all, int b STAMP_ARG) {
    const int tid = threadIdx.x, wid = __builtin_amdgcn_readfirstlane(tid >> 6), lane = tid & 63, fr = lane & 15, fq = lane >> 4;
    const int grp = b - 224, ch0 = grp * 32 + 24 + wid;
    LAS char* lu = lds_all + wid * 20480;
    LAS char* lh = lu + 4096;
    const unsigned lua = (unsigned)(size_t)lu;
    const int cpy = (fr + 1) & 3;
    const int alc = 2 * (fr - 15 + 8 * fq - cpy + (cpy ? 4 : 0));
    unsigned mk[4];
#pragma unroll
    for (int i = 0; i < 4; ++i) { const int e = fr - 15 + 8 * fq + 2 * i; mk[i] = (e >= 0 ? 0xffffu : 0u) | (e + 1 >= 0 ? 0xffff0000u : 0u); }
    LAS const char* lhc = lh + cpy * 4096; LAS const char* bb = lu + 16 * (254 - 224 - 2 * fr + fq);
    const unsigned v0 = (unsigned)lane * 16u, v1 = (unsigned)(lane ^ 4) * 16u + 2u, v2 = (unsigned)(lane ^ 8) * 16u + 4u, v3 = (unsigned)(lane ^ 12) * 16u + 6u;
    const unsigned vu = (unsigned)lane * 16384u;
    auto fetch_h = [&](int d) {
        const char* hr = d ? (const char*)(p->hk + (size_t)d * SEQ) : p->ws + WS_HK0;
#pragma unroll
        for (int i = 0; i < 4; ++i) {
            glds_asm(v0, hr + i * 1024, lua + 4096 + i * 1024); glds_asm(v2, hr + i * 1024 - 8, lua + 12288 + i * 1024);
            glds_asm(v1, hr + i * 1024 - 8, lua + 8192 + i * 1024); glds_asm(v3, hr + i * 1024 - 8, lua + 16384 + i * 1024);
        }
    };
    auto fetch_u = [&](int d) {
        const char* ur = p->ws + WS_KVT + (size_t)d * 16;
#pragma unroll
        for (int i = 0; i < 4; ++i) glds_asm(vu, ur + (size_t)i * 64 * 16384, lua + i * 1024);
    };
    unsigned dall;
    asm volatile(HZ "global_load_ushort %0, %1, %2" : "=v"(dall) : "v"((unsigned)(lane & 3) * 16u), "s"(p->dvec + ch0 - 24) : "memory");
    unsigned tag;
    {
        touch_asm((unsigned)((lane >> 5) * 8 * SEQ * 2 + (lane & 31) * 128), p->hk + (size_t)(ch0 - 8) * SEQ, (unsigned)(size_t)lds_all + 163584);
        touch_asm((unsigned)((lane >> 5) * 8 * SEQ * 2 + (lane & 31) * 128), p->hk + (size_t)(ch0 - 24) * SEQ, (unsigned)(size_t)lds_all + 163584);
        P0Regs pr; p0_load(p, pr, b, wid, lane);
        p0_wait<0>(pr);
        tag = __builtin_amdgcn_readfirstlane(pr.ep) + 1u;
        p0_kv(p->ws, pr, lds_all + 131072 + wid * 3072, lds_all + 49152 + wid * 2048, b, wid, lane);
        row_stats(p->ws, b * 8 + wid, lane, pr.xa, pr.xb, tag);
    }
    if (ch0 == 24) {
        u32x4 t[4];
#pragma unroll
        for (int i = 0; i < 4; ++i) t[i] = *(const u32x4*)((const char*)p->hk + i * 1024 + lane * 16);
#pragma unroll
        for (int i = 0; i < 4; ++i) *(u32x4*)(p->ws + WS_HK0 + i * 1024 + lane * 16) = t[i];
    }
    asm volatile("s_waitcnt vmcnt(0)" : "+v"(dall) :: "memory");
    flag_set_all(p->ws, WS_FKV + (size_t)((b & 7) * 256 + (b >> 3) * 8 + wid) * 4, tag);
    BAR();
    if (wid < 4) fetch_h(ch0);
    if (wid == 4) {
#pragma unroll 1
        for (int i = 0; i < 4; ++i) flag_wait(p->ws, WS_FKV + (size_t)((grp >> 2) * 256 + i * 64 + lane) * 4, true, tag);
        __builtin_amdgcn_fence(__ATOMIC_ACQUIRE, "agent");
        WAIT_V(0);
    }
    BAR();
    STAMP(1);
    __builtin_amdgcn_s_setprio(1);
    fetch_u(ch0);
    if (wid >= 4) fetch_h(ch0);
#pragma unroll 1
    for (int c = 0; c < 4; ++c) {
        const int d = ch0 - 8 * c;
        WAIT_V(0);
        f32x4 acc[8];
#pragma unroll
        for (int t = 0; t < 8; ++t) acc[t] = (f32x4){0.f, 0.f, 0.f, 0.f};
        bf16x8 A[2][8], B[2][8];
#define CV_LDB(r, i) B[(r) & 1][i] = *(const volatile LAS bf16x8*)(bb + 64 * (r) + 3584 - 512 * (i))
#define CV_LDA(r, i) do { const bf16x4 lo = *(const volatile LAS bf16x4*)(a0 + 512 * (i)), hi = *(const volatile LAS bf16x4*)(a1 + 512 * (i)); \
            A[(r) & 1][i] = __builtin_shufflevector(lo, hi, 0, 1, 2, 3, 4, 5, 6, 7); } while (0)
#define CV_ITEM(r, k) do { if ((k) == 0) CV_LDA(r, 0); else if ((k) < 8) CV_LDB(r, k); else if ((k) == 8) CV_LDB(r, 0); else CV_LDA(r, (k) - 8); } while (0)
        {
            LAS const char* a0 = lhc + (alc ^ (cpy << 6)); LAS const char* a1 = lhc + ((alc + 8) ^ (cpy << 6));
#pragma unroll
            for (int k = 0; k < 16; ++k) CV_ITEM(0, k);
            u32x4 t = __builtin_bit_cast(u32x4, A[0][0]); t.x &= mk[0]; t.y &= mk[1]; t.z &= mk[2]; t.w &= mk[3]; A[0][0] = __builtin_bit_cast(bf16x8, t);
        }
        __builtin_amdgcn_s_setprio(0);
#pragma unroll
        for (int r = 0; r < 8; ++r) {
            if (r == 1) __builtin_amdgcn_s_setprio(2);
            SCHED();
            LAS const char* a0 = lhc + ((64 * (r + 1) + alc) ^ (cpy << 6)); LAS const char* a1 = lhc + ((64 * (r + 1) + alc + 8) ^ (cpy << 6));
            int cnt = 0;
#pragma unroll
            for (int i = 0; i < 8; ++i) {
#pragma unroll
                for (int mm = 0; mm + i < 8; ++mm) {
                    const int m = (mm + 1 + i == 8) ? 0 : mm + 1;
                    if (i == 0 && m == 0) { if (2 * fr + 1 - fq - 4 * r < 0) B[r & 1][0] = (bf16x8){0, 0, 0, 0, 0, 0, 0, 0}; }
                    acc[i + m] = __builtin_amdgcn_mfma_f32_16x16x32_bf16(A[r & 1][i], B[r & 1][m], acc[i + m], 0, 0, 0);
                    if ((cnt & 1) == 1) { if (r < 7 && cnt < 32) CV_ITEM(r + 1, cnt >> 1); SCHED(); }
                    ++cnt;
                }
            }
        }
#undef CV_LDA
#undef CV_LDB
#undef CV_ITEM
        __builtin_amdgcn_s_setprio(1);
        u32x2 uu[8];
#pragma unroll
        for (int t = 0; t < 8; ++t) uu[t] = *(const volatile LAS u32x2*)(lu + 2 * (2044 - (256 * t + 16 * fr + 4 * fq)));
        asm volatile("s_waitcnt lgkmcnt(0)" : "+v"(uu[0]), "+v"(uu[1]), "+v"(uu[2]), "+v"(uu[3]), "+v"(uu[4]), "+v"(uu[5]), "+v"(uu[6]), "+v"(uu[7]) :: "memory");
        if (c < 3) { fetch_u(d - 8); fetch_h(d - 8); }
        const float dd = __uint_as_float((unsigned)__builtin_amdgcn_readlane((int)dall, 3 - c) << 16);
        char* zp = p->ws + WS_ZT + (size_t)d * SEQ * 2;
        const unsigned zv = (unsigned)(16 * fr + 4 * fq) * 2u;
#pragma unroll
        for (int t = 0; t < 8; ++t) {
            const float z0 = fma_s(bf_hi(uu[t].y), dd, acc[t][0]), z1 = fma_s(bf_lo(uu[t].y), dd, acc[t][1]), z2 = fma_s(bf_hi(uu[t].x), dd, acc[t][2]), z3 = fma_s(bf_lo(uu[t].x), dd, acc[t][3]);
            gstore2_sc1(zv, zp + 512 * t, (u32x2){pk_bf16(z0, z1), pk_bf16(z2, z3)});
        }
    }
    __builtin_amdgcn_s_setprio(0);
    STAMP(2);
    WAIT_V(0); BAR();
    if (tid == 0) flag_set(p->ws, WS_FZ + grp * 4, tag);
    {
        const char* wB = g2_baseB(p, g2_pos(b)); const unsigned wv = g2_voffB(wid, lane);
#pragma unroll
        for (int t = 0; t < 6; ++t) g2_fill(wB, wv, (unsigned)(size_t)lds_all + wid * 1024, t);
    }
    WAIT_V(0);
    return tag;
}

template <int N> __device__ __forceinline__ void wait_v4(u32x4 (&a)[4]) { asm volatile("s_waitcnt vmcnt(%4)" : "+v"(a[0]), "+v"(a[1]), "+v"(a[2]), "+v"(a[3]) : "n"(N) : "memory"); }

__device__ __forceinline__ void gemm2_body(KP p, LAS char* lds, int b, unsigned tag STAMP_ARG) {
    int tid = threadIdx.x; asm volatile("" : "+v"(tid));
    const int wid = __builtin_amdgcn_readfirstlane(tid >> 6), lane = tid & 63, wm = wid & 3, kp = wid >> 2, fr = lane & 15, fq = lane >> 4;
    const unsigned lds0 = (unsigned)(size_t)lds;
    const G2Pos g = g2_pos(b);
    const unsigned voffB = g2_voffB(wid, lane);
    const unsigned voffA0 = (unsigned)lane * 16u, voffA1 = voffA0 + (FF / 32) * 1024u;
    const char* cA = p->ws + WS_H + ((size_t)(g.tm * 8 + wm * 2) * (FF / 32) + g.kh * (FF / 64) + kp) * 1024;
    const char* cB = g2_baseB(p, g);
    const unsigned ldsw = lds0 + (unsigned)wid * 1024u;
    const int tq = (lane & 15) >> 2, tp = lane & 3;
    const int r0 = g.tm * 128 + g.kh * 64, c0 = g.tn * 128;
    if (wid == 0) flag_wait(p->ws, WS_FH + (size_t)((g.tm >> 1) * 28 + g.kh * 14 + (lane < 14 ? lane : 0)) * 4, true, tag);
    BAR();
    STAMP2(4);
    u32x4 ar[3][4];
#define G2_LDA(set, P) do { const char* _a = cA + (size_t)(P) * 4096; gload_sc1(ar[set][0], voffA0, _a); gload_sc1(ar[set][1], voffA1, _a); \
        gload_sc1(ar[set][2], voffA0, _a + 2048); gload_sc1(ar[set][3], voffA1, _a + 2048); } while (0)
    G2_LDA(0, 0); G2_LDA(1, 1);
    LAS char* lz = lds + 131072; LAS char* lx = lz + 16384;
    {
        const unsigned xoff = (unsigned)((wid * 4 + (lane >> 4)) * DM + (((lane & 15) ^ ((wid * 4 + (lane >> 4)) & 15)) * 8)) * 2u;
        const char* xg = (const char*)p->x + ((size_t)r0 * DM + c0) * 2;
        glds_asm(xoff, xg, ldsw + 147456); glds_asm(xoff, xg + (size_t)32 * DM * 2, ldsw + 147456 + 8192);
    }
    int boff[4];
#pragma unroll
    for (int n = 0; n < 4; ++n) boff[n] = (32 * kp + 8 * fq + tq) * 256 + (((2 * n + (tp >> 1)) ^ ((tq << 1) | ((fq & 1) << 3))) * 16) + 8 * (tp & 1);
    f32x4 acc[2][8];
#pragma unroll
    for (int m = 0; m < 2; ++m)
#pragma unroll
        for (int n = 0; n < 8; ++n) acc[m][n] = (f32x4){0.f, 0.f, 0.f, 0.f};
    bf16x8 bt[2][8];
    LAS const char* lb[2][8];
#pragma unroll
    for (int n = 0; n < 8; ++n) { lb[0][n] = lds + (boff[n & 3] ^ ((n >> 2) * 128)); lb[1][n] = lb[0][n] + 65536; asm volatile("" : "+v"(lb[0][n]), "+v"(lb[1][n])); }
    __builtin_amdgcn_s_setprio(G2P_LD);
    if (kp == 1) BAR();
#define G2_RD(P, s, n) bt[s][n] = tr_frag(lb[((P) >> 1) & 1][n] + ((P) & 1) * 32768 + (s) * 16384, 1024)
#define G2_PHASE(set, P, DOB, DOA, W, HOOK) do { \
        const char* _a = cA + (size_t)((P) + 2) * 4096; const char* _b = cB + (size_t)(2 * (P) + 6) * G2_BSTEP; const unsigned _l = ldsw + (((P) + 3) & 3) * 32768; \
        _Pragma("unroll") for (int i = 0; i < 4; ++i) { \
            if (DOA) gload_sc1(ar[((set) + 2) % 3][i], (i & 1) ? voffA1 : voffA0, _a + (i >> 1) * 2048); \
            G2_RD(P, 0, 2 * i); G2_RD(P, 0, 2 * i + 1); \
        } \
        HOOK; \
        wait_v4<W>(ar[set]); WAIT_L(0); BAR(); \
        __builtin_amdgcn_s_setprio(G2P_MM); \
        _Pragma("unroll") for (int n = 0; n < 8; ++n) { \
            acc[0][n] = __builtin_amdgcn_mfma_f32_16x16x32_bf16(bt[0][n], __builtin_bit_cast(bf16x8, ar[set][0]), acc[0][n], 0, 0, 0); SCHED(); \
            G2_RD(P, 1, n); SCHED(); \
            acc[1][n] = __builtin_amdgcn_mfma_f32_16x16x32_bf16(bt[0][n], __builtin_bit_cast(bf16x8, ar[set][1]), acc[1][n], 0, 0, 0); SCHED(); \
        } \
        _Pragma("unroll") for (int n = 0; n < 8; ++n) { \
            acc[0][n] = __builtin_amdgcn_mfma_f32_16x16x32_bf16(bt[1][n], __builtin_bit_cast(bf16x8, ar[set][2]), acc[0][n], 0, 0, 0); \
            acc[1][n] = __builtin_amdgcn_mfma_f32_16x16x32_bf16(bt[1][n], __builtin_bit_cast(bf16x8, ar[set][3]), acc[1][n], 0, 0, 0); SCHED(); \
            if ((n & 1) == 0 && DOB) { glds_asm(voffB, _b + (n >> 1) * (G2_BSTEP / 2), _l + (n >> 1) * 8192); SCHED(); } \
        } \
        __builtin_amdgcn_s_setprio(G2P_LD); \
        BAR(); SCHED(); } while (0)
    G2_PHASE(0, 0, 1, 1, 10, {});
    G2_PHASE(1, 1, 1, 1, 12, {});
    G2_PHASE(2, 2, 1, 1, 12, {});
    G2_PHASE(0, 3, 1, 1, 12, {});
    GSTAMP(5);
    G2_PHASE(1, 4, 1, 1, 12, { GSTAMP(6); });
    GSTAMP(7);
    G2_PHASE(2, 5, 1, 1, 12, {});
    G2_PHASE(0, 6, 1, 1, 12, {});
    G2_PHASE(1, 7, 1, 1, 12, {});
    unsigned zf;
    G2_PHASE(2, 8, 1, 1, 13, { asm volatile(HZ "global_load_dword %0, %1, %2 sc1" : "=v"(zf) : "v"((unsigned)(lane & 3) * 4u), "s"(p->ws + WS_FZ + g.tn * 16) : "memory"); });
    G2_PHASE(0, 9, 1, 1, 13, {});
    G2_PHASE(1, 10, 1, 1, 12, {});
    u32x4 zr[2];
    G2_PHASE(2, 11, 0, 1, 12, {});
    G2_PHASE(0, 12, 0, 0, 6, {
        asm volatile("" : "+v"(zf));
        if (!__all(zf == tag)) flag_wait(p->ws, WS_FZ + (size_t)(g.tn * 4 + (lane & 3)) * 4, true, tag);
        const unsigned zoff = (unsigned)((wid * 8 + (lane >> 3)) * SEQ + (lane & 7) * 8) * 2u;
        const char* zg = p->ws + WS_ZT + ((size_t)c0 * SEQ + r0) * 2;
        gload_sc1(zr[0], zoff, zg); gload_sc1(zr[1], zoff, zg + (size_t)64 * SEQ * 2); });
    G2_PHASE(1, 13, 0, 0, 0, {
        asm volatile("s_waitcnt vmcnt(0)" : "+v"(zr[0]), "+v"(zr[1]) :: "memory");
        const int zrow = wid * 8 + (lane >> 3);
        LAS char* zw = lz + zrow * 128 + (((lane & 7) ^ (zrow & 6)) * 16);
        *(LAS u32x4*)zw = zr[0]; *(LAS u32x4*)(zw + 8192) = zr[1]; });
#undef G2_PHASE
#undef G2_RD
#undef G2_LDA
    if (kp == 0) BAR();
    STAMP2(5);
    f32x4 sum[8];
    {
        LAS char* ex = lds + (wid ^ 4) * 8192 + lane * 16;
#pragma unroll
        for (int n = 0; n < 8; ++n) {
            f32x4 snd;
#pragma unroll
            for (int j = 0; j < 4; ++j) { const float lo = acc[0][n][j], hi = acc[1][n][j]; snd[j] = kp ? lo : hi; sum[n][j] = kp ? hi : lo; }
            *(LAS f32x4*)(ex + n * 1024) = snd;
        }
        WAIT_L(0); BAR();
#pragma unroll
        for (int n = 0; n < 8; ++n) sum[n] += *(const LAS f32x4*)(lds + wid * 8192 + n * 1024 + lane * 16);
    }
    const int slot = (wm & 1) * 2 + kp;
    if ((wm >> 1) != g.kh) {
        char* xo = p->ws + WS_EX + ((size_t)b * 4 + slot) * 8192;
#pragma unroll
        for (int n = 0; n < 8; ++n) gstore_sc1((unsigned)lane * 16u, xo + n * 1024, __builtin_bit_cast(u32x4, sum[n]));
        WAIT_V(0);
        if (lane == 0) flag_set(p->ws, WS_FX + ((size_t)b * 4 + slot) * 4, tag);
    } else {
        const int xr = slot * 16 + fr;
        const unsigned sv = granule_wait(p->ws, WS_STATS + (size_t)(r0 + xr) * 8, tag);
        const float mu = bf_lo(sv), rs = 1.0f / bf_hi(sv);
#pragma unroll
        for (int n = 0; n < 8; ++n) {
            const int zrow = n * 16 + 4 * fq + tq, zc = 2 * slot + (tp >> 1);
            const u32x2 zw = __builtin_bit_cast(u32x2, __builtin_amdgcn_ds_read_tr16_b64_v4i16((LAS bf16x4*)(lz + zrow * 128 + ((zc ^ (zrow & 6)) * 16) + 8 * (tp & 1))));
            const u32x2 xv = *(const LAS u32x2*)(lx + xr * 256 + (((2 * n + (fq >> 1)) ^ (xr & 15)) * 16) + 8 * (fq & 1));
            const float x0 = bf_lo(xv.x), x1 = bf_hi(xv.x), x2 = bf_lo(xv.y), x3 = bf_hi(xv.y);
            sum[n][0] += x0 + rnd_bf(rnd_bf(x0 - mu) * rs) * bf_lo(zw.x); sum[n][1] += x1 + rnd_bf(rnd_bf(x1 - mu) * rs) * bf_hi(zw.x);
            sum[n][2] += x2 + rnd_bf(rnd_bf(x2 - mu) * rs) * bf_lo(zw.y); sum[n][3] += x3 + rnd_bf(rnd_bf(x3 - mu) * rs) * bf_hi(zw.y);
        }
        flag_wait(p->ws, WS_FX + ((size_t)(b ^ 1) * 4 + slot) * 4, true, tag);
        STAMP2(6);
        u32x4 in[8];
#pragma unroll
        for (int n = 0; n < 8; ++n) gload_sc1(in[n], (unsigned)lane * 16u, p->ws + WS_EX + ((size_t)(b ^ 1) * 4 + slot) * 8192 + n * 1024);
        asm volatile("s_waitcnt vmcnt(0)" : "+v"(in[0]), "+v"(in[1]), "+v"(in[2]), "+v"(in[3]), "+v"(in[4]), "+v"(in[5]), "+v"(in[6]), "+v"(in[7]) :: "memory");
        u16* op = p->out + (size_t)(r0 + xr) * DM + c0 + (((fq & 1) << 1) | (fq >> 1)) * 8;
#pragma unroll
        for (int h = 0; h < 4; ++h) {
            const f32x4 u = sum[2 * h] + __builtin_bit_cast(f32x4, in[2 * h]), v = sum[2 * h + 1] + __builtin_bit_cast(f32x4, in[2 * h + 1]);
            const auto q0 = __builtin_amdgcn_permlane16_swap(pk_bf16(u[0], u[1]), pk_bf16(v[0], v[1]), false, false);
            const auto q1 = __builtin_amdgcn_permlane16_swap(pk_bf16(u[2], u[3]), pk_bf16(v[2], v[3]), false, false);
            *(u32x4*)(op + h * 32) = (u32x4){q0[0], q1[0], q0[1], q1[1]};
        }
    }
}

__global__ __launch_bounds__(512, 2) void fused_kernel(Params args) {
    KP p = (KP)__builtin_amdgcn_kernarg_segment_ptr();
    extern __shared__ __attribute__((aligned(16))) char smem[];
    LAS char* lds = (LAS char*)smem;
    const int b = blockIdx.x;
    STAMP3(0);
    { const int wid = __builtin_amdgcn_readfirstlane(threadIdx.x >> 6);
      p0_stage(p, (unsigned)(size_t)lds + 131072 + wid * 3072, (unsigned)(size_t)lds + 49152 + wid * 2048, b, wid, threadIdx.x & 63);
    }
    const unsigned tag = b < 224 ? gemm1_body(p, lds, b STAMP_PASS) : conv_body(p, lds, b STAMP_PASS);
    STAMP2(3);
    gemm2_body(fresh(p), lds, b, tag STAMP_PASS);
    p = fresh(p);
    if (b == 0 && threadIdx.x < 64) {
        u32x4 f; gload_sc1(f, threadIdx.x * 16u, p->ws + WS_FH);
        asm volatile("s_waitcnt vmcnt(0)" : "+v"(f) :: "memory");
        if (!__all(f.x == tag && f.y == tag && f.z == tag && f.w == tag)) {
#pragma unroll 1
            for (int i = 0; i < 4; ++i) flag_wait(p->ws, WS_FH + (size_t)(i * 64 + threadIdx.x) * 4, true, tag);
        }
        if (threadIdx.x == 0) flag_set(p->ws, WS_EPOCH, tag);
    }
}

extern "C" void kernel_launch(void* const* d_in, const int* in_sizes, int n_in, void* d_out, int out_size, void* d_ws, size_t ws_size, hipStream_t stream) {
    Params p;
    p.x = (const u16*)d_in[0]; p.kk = (const u16*)d_in[1]; p.vv = (const u16*)d_in[2]; p.shk = (const u16*)d_in[3]; p.hk = (const u16*)d_in[4]; p.dvec = (const u16*)d_in[5];
    p.wg = (const u16*)d_in[7]; p.wu = (const u16*)d_in[8]; p.wd = (const u16*)d_in[9]; p.out = (u16*)d_out; p.ws = (char*)d_ws;
    constexpr int LDS_BYTES = 163840;
    (void)hipFuncSetAttribute((const void*)fused_kernel, hipFuncAttributeMaxDynamicSharedMemorySize, LDS_BYTES);
    fused_kernel<<<256, 512, LDS_BYTES, stream>>>(p);
}
```

```cpp
#include <hip/hip_runtime.h>
#include <stdint.h>

typedef unsigned short u16;
typedef short bf16x8 __attribute__((ext_vector_type(8)));
typedef short bf16x4 __attribute__((ext_vector_type(4)));
typedef float f32x4 __attribute__((ext_vector_type(4)));
typedef float f32x2 __attribute__((ext_vector_type(2)));
typedef __bf16 bfx2 __attribute__((ext_vector_type(2)));
typedef unsigned u32x4 __attribute__((ext_vector_type(4)));
typedef unsigned u32x2 __attribute__((ext_vector_type(2)));
#define LAS __attribute__((address_space(3)))
#define GAS __attribute__((address_space(1)))

constexpr int SEQ = 2048, DM = 1024, FF = 3584;
constexpr int HRB = 29 * 4096;
constexpr int KVS = 5 * 4096;
constexpr int FL = 128;
constexpr size_t WS_EPOCH = 0, WS_FH = (1u << 20) + 65536, WS_FZ = WS_FH + 224 * FL, WS_FX = WS_FH + 65536, WS_TMO = 8192, WS_FKV = 16384, WS_FZ2 = 24576;
constexpr size_t WS_STATS = 32768, WS_CS = 65536 + 32768, WS_HK0 = (1u << 20) + 49152, WS_KVT = (2u << 20), WS_ZT = (8u << 20), WS_H = (12u << 20), WS_EX = (40u << 20);

__device__ __forceinline__ size_t cs_off(int pn) { return (size_t)(pn >> 1) * 65536 + (pn & 1) * 16384; }
__device__ __forceinline__ unsigned pk_bf16(float a, float b) { f32x2 v = {a, b}; bfx2 r = __builtin_convertvector(v, bfx2); return __builtin_bit_cast(unsigned, r); }
__device__ __forceinline__ float bf_lo(unsigned w) { return __uint_as_float(w << 16); }
__device__ __forceinline__ float bf_hi(unsigned w) { return __uint_as_float(w & 0xffff0000u); }
__device__ __forceinline__ float rnd_bf(float a) { return bf_lo(pk_bf16(a, a)); }

#define WAIT_V(n) asm volatile("s_waitcnt vmcnt(" #n ")" ::: "memory")
#define WAIT_L(n) asm volatile("s_waitcnt lgkmcnt(" #n ")" ::: "memory")
#define BAR() do { asm volatile("" ::: "memory"); __builtin_amdgcn_s_barrier(); asm volatile("" ::: "memory"); } while (0)
#define SCHED() __builtin_amdgcn_sched_barrier(0)

__device__ __forceinline__ void glds_asm(unsigned voff, const void* sbase, unsigned ldsaddr) {
    asm volatile("s_mov_b32 m0, %2\n\ts_nop 0\n\tglobal_load_lds_dwordx4 %0, %1" :: "v"(voff), "s"(sbase), "s"(ldsaddr) : "memory");
}
__device__ __forceinline__ void touch_asm(unsigned voff, const void* sbase, unsigned ldsaddr) {
    asm volatile("s_mov_b32 m0, %2\n\ts_nop 0\n\tglobal_load_lds_dword %0, %1" :: "v"(voff), "s"(sbase), "s"(ldsaddr) : "memory");
}
__device__ __forceinline__ void gload4(u32x4& dst, unsigned voff, const void* sbase) {
    asm volatile("global_load_dwordx4 %0, %1, %2" : "=v"(dst) : "v"(voff), "s"(sbase) : "memory");
}
__device__ __forceinline__ void gload_sc1(u32x4& dst, unsigned voff, const void* sbase) {
    asm volatile("global_load_dwordx4 %0, %1, %2 sc1" : "=v"(dst) : "v"(voff), "s"(sbase) : "memory");
}
__device__ __forceinline__ void gload2_sc1(u32x2& dst, unsigned voff, const void* sbase) {
    asm volatile("global_load_dwordx2 %0, %1, %2 sc1" : "=v"(dst) : "v"(voff), "s"(sbase) : "memory");
}
__device__ __forceinline__ void gstore_sc1(unsigned voff, void* sbase, u32x4 v) {
    asm volatile("global_store_dwordx4 %0, %1, %2 sc1\n\ts_nop 1" :: "v"(voff), "v"(v), "s"(sbase) : "memory");
}
__device__ __forceinline__ void gstore_sc1_free(unsigned voff, void* sbase, u32x4 v) {
    asm volatile("global_store_dwordx4 %0, %1, %2 sc1\n\ts_nop 1" :: "v"(voff), "v"(v), "s"(sbase));
}
__device__ __forceinline__ void gstore2_sc1(unsigned voff, void* sbase, u32x2 v) {
    asm volatile("global_store_dwordx2 %0, %1, %2 sc1" :: "v"(voff), "v"(v), "s"(sbase) : "memory");
}
__device__ __forceinline__ void gstore2_sc1_half(unsigned voff, void* sbase, u32x2 v) {
    asm volatile("s_mov_b32 exec_lo, 0x00ff00ff\n\ts_mov_b32 exec_hi, 0x00ff00ff\n\tglobal_store_dwordx2 %0, %1, %2 sc1\n\ts_mov_b64 exec, -1" :: "v"(voff), "v"(v), "s"(sbase) : "memory");
}
__device__ __forceinline__ float fma_s(float a, float b, float c) { float r; asm("v_fma_f32 %0, %1, %2, %3" : "=v"(r) : "v"(a), "s"(b), "v"(c)); return r; }
__device__ __forceinline__ float sub_u(float a, float b) { float r; asm("v_sub_f32 %0, %1, %2" : "=v"(r) : "v"(a), "v"(b)); return r; }

__device__ __forceinline__ bf16x8 tr_frag(LAS const char* p, int off_t1) {
    const bf16x4 a = __builtin_amdgcn_ds_read_tr16_b64_v4i16((LAS bf16x4*)p);
    const bf16x4 b = __builtin_amdgcn_ds_read_tr16_b64_v4i16((LAS bf16x4*)(p + off_t1));
    return __builtin_shufflevector(a, b, 0, 1, 2, 3, 4, 5, 6, 7);
}

__device__ __forceinline__ void flag_set(char* ws, size_t off, unsigned tag) { __hip_atomic_store((GAS unsigned*)(ws + off), tag, __ATOMIC_RELAXED, __HIP_MEMORY_SCOPE_AGENT); }
__device__ __forceinline__ void flag_wait(char* ws, size_t off, bool active, unsigned tag) {
    const unsigned long long t0 = __builtin_amdgcn_s_memrealtime();
    for (;;) {
        const unsigned v = active ? __hip_atomic_load((GAS unsigned*)(ws + off), __ATOMIC_RELAXED, __HIP_MEMORY_SCOPE_AGENT) : tag;
        if (__all(v == tag)) break;
        if (__builtin_amdgcn_s_memrealtime() - t0 > 2000000ull) { __hip_atomic_store((GAS unsigned*)(ws + WS_TMO), tag, __ATOMIC_RELAXED, __HIP_MEMORY_SCOPE_AGENT); break; }
        __builtin_amdgcn_s_sleep(4);
    }
}
__device__ __forceinline__ unsigned granule_wait(char* ws, size_t off, unsigned tag) {
    const unsigned long long t0 = __builtin_amdgcn_s_memrealtime();
    for (;;) {
        const unsigned long long g = __hip_atomic_load((GAS unsigned long long*)(ws + off), __ATOMIC_RELAXED, __HIP_MEMORY_SCOPE_AGENT);
        if ((unsigned)(g >> 32) == tag) return (unsigned)g;
        if (__builtin_amdgcn_s_memrealtime() - t0 > 2000000ull) { __hip_atomic_store((GAS unsigned*)(ws + WS_TMO), tag, __ATOMIC_RELAXED, __HIP_MEMORY_SCOPE_AGENT); return (unsigned)g; }
    }
}

template <int CTRL, int RMASK> __device__ __forceinline__ float dpp_add(float v) {
    return v + __builtin_bit_cast(float, __builtin_amdgcn_update_dpp(0, __builtin_bit_cast(int, v), CTRL, RMASK, 0xf, false));
}
__device__ __forceinline__ void wave_sum2(float& a, float& b) {
    a = dpp_add<0x111, 0xf>(a); b = dpp_add<0x111, 0xf>(b); a = dpp_add<0x112, 0xf>(a); b = dpp_add<0x112, 0xf>(b);
    a = dpp_add<0x114, 0xf>(a); b = dpp_add<0x114, 0xf>(b); a = dpp_add<0x118, 0xf>(a); b = dpp_add<0x118, 0xf>(b);
    a = dpp_add<0x142, 0xa>(a); b = dpp_add<0x142, 0xa>(b); a = dpp_add<0x143, 0xc>(a); b = dpp_add<0x143, 0xc>(b);
    a = __builtin_bit_cast(float, __builtin_amdgcn_readlane(__builtin_bit_cast(int, a), 63)); b = __builtin_bit_cast(float, __builtin_amdgcn_readlane(__builtin_bit_cast(int, b), 63));
}

struct Params { const u16 *x, *kk, *vv, *shk, *hk, *dvec, *wg, *wu, *wd; u16* out; char* ws; };
typedef const __attribute__((address_space(4))) Params* KP;
__device__ __forceinline__ KP fresh(KP q) { asm volatile("" : "+s"(q)); return q; }
__device__ __forceinline__ int fresh_s(int v) { asm volatile("" : "+s"(v)); return v; }
__device__ __forceinline__ int fresh(int v) { asm volatile("" : "+v"(v)); return v; }

struct P0Regs { unsigned ep; u32x4 tw, xa, xb; };
__device__ __forceinline__ void p0_stage(KP p, unsigned lk, unsigned lv, int b, int wid, int lane) {
    const int rb = (b >> 3) * 8 + wid;
    const u16* pk = p->kk + (size_t)rb * 8 * DM + (b & 7) * 128; const u16* pv = p->vv + (size_t)rb * 8 * DM + (b & 7) * 128;
    const unsigned vr = (unsigned)((lane >> 4) * DM * 2 + (lane & 15) * 16);
    glds_asm(vr, pk + (rb ? -3 * DM : 0), lk); glds_asm(vr, pk + DM, lk + 1024); glds_asm(vr, pk + 4 * DM, lk + 1792);
    glds_asm(vr, pv, lv); glds_asm(vr, pv + 4 * DM, lv + 1024);
}
template <bool X> __device__ __forceinline__ void p0_load(KP p, P0Regs& r, int b, int wid, int lane) {
    const u16* px = p->x + (size_t)(b * 8 + wid) * DM; const unsigned v16 = (unsigned)lane * 16u;
    gload4(r.tw, v16, p->shk + (b & 7) * 512);
    if (X) { gload4(r.xa, v16, px); gload4(r.xb, v16, px + 512); } else { r.xa = (u32x4){0u, 0u, 0u, 0u}; r.xb = r.xa; }
    asm volatile("global_load_dword %0, %1, %2 sc1" : "=v"(r.ep) : "v"(0u), "s"(p->ws + WS_EPOCH) : "memory");
}
template <int N> __device__ __forceinline__ void p0_wait(P0Regs& r) { asm volatile("s_waitcnt vmcnt(%4)" : "+v"(r.ep), "+v"(r.tw), "+v"(r.xa), "+v"(r.xb) : "n"(N) : "memory"); }
__device__ __forceinline__ void p0_kv(char* ws, const P0Regs& r, LAS const char* lk, LAS const char* lv, int b, int wid, int lane) {
    const f32x2 w0 = {bf_lo(r.tw.x), bf_lo(r.tw.z)}, w1 = {bf_hi(r.tw.x), bf_hi(r.tw.z)}, w2 = {bf_lo(r.tw.y), bf_lo(r.tw.w)}, w3 = {bf_hi(r.tw.y), bf_hi(r.tw.w)};
    const int rb = (b >> 3) * 8 + wid;
    f32x2 kf[11], o[8];
#pragma unroll
    for (int i = 0; i < 11; ++i) {
        unsigned w = *(const LAS unsigned*)(lk + i * 256 + lane * 4);
        if (i < 4 && rb == 0) w = i == 3 ? *(const LAS unsigned*)(lk + lane * 4) : 0u;
        kf[i] = (f32x2){bf_lo(w), bf_hi(w)};
    }
#pragma unroll
    for (int i = 0; i < 8; ++i) { const unsigned v = *(const LAS unsigned*)(lv + i * 256 + lane * 4); o[i] = (kf[i + 3] * w0 + (kf[i + 2] * w1 + (kf[i + 1] * w2 + kf[i] * w3))) * (f32x2){bf_lo(v), bf_hi(v)}; }
#pragma unroll
    for (int e = 0; e < 2; ++e)
        gstore_sc1((unsigned)lane * 32u, ws + WS_KVT + (size_t)(255 - rb) * KVS + (b & 7) * 2048 + e * 16,
                   (u32x4){pk_bf16(o[7][e], o[6][e]), pk_bf16(o[5][e], o[4][e]), pk_bf16(o[3][e], o[2][e]), pk_bf16(o[1][e], o[0][e])});
}
__device__ __forceinline__ void flag_set_1(unsigned zero, unsigned tagv, char* addr) {
    asm volatile("s_mov_b64 exec, 1\n\tglobal_store_dword %0, %1, %2 sc1\n\ts_mov_b64 exec, -1" :: "v"(zero), "v"(tagv), "s"(addr) : "memory");
}
__device__ __forceinline__ void flag_set_lane0(char* ws, size_t off, unsigned tag) { flag_set_1(0u, tag, ws + off); }
__device__ __forceinline__ void kv_wait(char* ws, int blk, int lane, unsigned tag) {
    const unsigned long long t0 = __builtin_amdgcn_s_memrealtime();
    for (;;) {
        u32x4 f; gload_sc1(f, (unsigned)lane * 16u, ws + WS_FKV + (size_t)blk * 1024);
        asm volatile("s_waitcnt vmcnt(0)" : "+v"(f) :: "memory");
        if (__all(f.x == tag && f.y == tag && f.z == tag && f.w == tag)) break;
        if (__builtin_amdgcn_s_memrealtime() - t0 > 2000000ull) { flag_set(ws, WS_TMO, tag); break; }
        __builtin_amdgcn_s_sleep(8);
    }
}
__device__ __forceinline__ void row_sums(u32x4 xa, u32x4 xb, float& c, float& a1, float& a2) {
    c = __builtin_bit_cast(float, __builtin_amdgcn_readfirstlane((int)(xa.x << 16)));
    a1 = 0.f; a2 = 0.f;
#pragma unroll
    for (int i = 0; i < 16; ++i) { const unsigned w = i < 8 ? xa[i >> 1] : xb[(i - 8) >> 1]; const float d = sub_u((i & 1) ? bf_hi(w) : bf_lo(w), c); a1 += d; a2 = __builtin_fmaf(d, d, a2); }
    wave_sum2(a1, a2);
}
__device__ __forceinline__ void row_publish(char* ws, int row, float c, float a1, float a2, unsigned tag) {
    const float dm = a1 * (1.0f / DM), mean = c + dm, var = __builtin_fmaxf(a2 * (1.0f / DM) - dm * dm, 0.f);
    const float sd = __builtin_amdgcn_sqrtf(rnd_bf(rnd_bf(var) + 1e-5f));
    asm volatile("s_mov_b64 exec, 1\n\tglobal_store_dwordx2 %0, %1, %2 sc1\n\ts_mov_b64 exec, -1" :: "v"(0u), "v"((u32x2){pk_bf16(mean, sd), tag}), "s"(ws + WS_STATS + (size_t)row * 8) : "memory");
}

constexpr size_t G2_BSTEP = (size_t)64 * DM * 2;
struct G2Pos { int tm, tn, kh; };
__device__ __forceinline__ G2Pos g2_pos(int b) { const int xcd = b & 7, idx = b >> 3; return G2Pos{(xcd >> 1) * 4 + (idx & 3), idx >> 2, xcd & 1}; }
__device__ __forceinline__ unsigned g2_voffB(int wid, int lane) { return (unsigned)((wid * 4 + (lane >> 4)) * DM + (((lane & 15) ^ (((lane >> 4) << 1) | ((wid & 2) << 2))) * 8)) * 2u; }
__device__ __forceinline__ const char* g2_baseB(KP p, const G2Pos& g) {
    return (const char*)p->wd + ((size_t)g.kh * (FF / 2) * DM + g.tn * 128) * 2;
}
__device__ __forceinline__ void g2_fill(const char* cB, unsigned voffB, unsigned ldsw, int t) {
    glds_asm(voffB, cB + (size_t)t * G2_BSTEP, ldsw + (t & 7) * 16384); glds_asm(voffB, cB + (size_t)t * G2_BSTEP + G2_BSTEP / 2, ldsw + (t & 7) * 16384 + 8192);
}

__device__ __forceinline__ void conv_fetch_h(const char* hr, unsigned lh, int lane, int j) {
    const int i = j >> 2, c = j & 3;
    glds_asm((unsigned)(lane ^ (4 * c)) * 16u + 2u * c, hr + i * 1024 - (c ? 8 : 0), lh + c * 4096 + i * 1024);
}
__device__ __forceinline__ const char* conv_row(KP p, int d) { return d ? (const char*)(p->hk + (size_t)d * SEQ) : p->ws + WS_HK0; }
__device__ __forceinline__ int coop_channel(int b) { return (b >> 3) * 32 + (b & 7); }
constexpr int CO = 131072;

__device__ __forceinline__ unsigned gemm1_body(KP p, LAS char* lds, int b) {
    const int tid = threadIdx.x, wid = __builtin_amdgcn_readfirstlane(tid >> 6), lane = tid & 63, wr = wid >> 2, wc = wid & 3, fr = lane & 15, fq = lane >> 4;
    const int xcd = b & 7, idx = b >> 3;
    const int pm = (xcd & 1) * 4 + (idx & 3), pn = (xcd >> 1) * 7 + (idx >> 2);
    const int rot = (pm >> 2) * 8;
    constexpr int HT = 16384, NT = DM / 64;
    const unsigned lds0 = (unsigned)(size_t)lds;
    const unsigned voffA = (unsigned)((wid * 8 + (lane >> 3)) * DM + (((lane & 7) ^ (lane >> 3)) * 8)) * 2u;
    const unsigned voffB = (unsigned)((wid * 4 + (lane >> 4)) * FF + (((lane & 15) ^ (((lane >> 4) << 1) | ((wid & 2) << 2))) * 8)) * 2u;
    const unsigned ldsw = lds0 + (unsigned)wid * 1024u;
    const char* cA = (const char*)p->x + (size_t)pm * 256 * DM * 2;
    const char* cG = (const char*)p->wg + (size_t)pn * 128 * 2;
    const char* cU = (const char*)p->wu + (size_t)pn * 128 * 2;
#define SA(b, h) (((b) * 2 + (h)) * HT)
#define SB(b, h) ((4 + (b) * 2 + (h)) * HT)
#define STAGE_A(b, h, t) do { const char* _g = cA + (size_t)(h) * 128 * DM * 2 + (size_t)(((t) + rot) & 15) * 128; \
        glds_asm(voffA, _g, ldsw + SA(b, h)); glds_asm(voffA, _g + 64 * DM * 2, ldsw + SA(b, h) + 8192); } while (0)
#define STAGE_B(b, h, t) do { const char* _g = ((h) ? cU : cG) + (size_t)(((t) + rot) & 15) * 64 * FF * 2; \
        glds_asm(voffB, _g, ldsw + SB(b, h)); glds_asm(voffB, _g + 32 * FF * 2, ldsw + SB(b, h) + 8192); } while (0)
    const int aoff0 = (wr * 64 + fr) * 128 + ((fq ^ (fr & 7)) * 16), aoff1 = aoff0 ^ 64;
    const int tq = (lane & 15) >> 2, tp = lane & 3;
    const int boff0 = (8 * fq + tq) * 256 + (((4 * wc + (tp >> 1)) ^ ((tq << 1) | ((fq & 1) << 3))) * 16) + 8 * (tp & 1), boff1 = boff0 ^ 32;
#define LDA(dst, b, h) do { _Pragma("unroll") for (int m = 0; m < 4; ++m) { \
        dst[m][0] = *(const LAS bf16x8*)(la0 + (SA(b, h) + m * 2048)); dst[m][1] = *(const LAS bf16x8*)(la1 + (SA(b, h) + m * 2048)); } } while (0)
    LAS const char* lb0 = lds + 65536 + boff0; LAS const char* lb1 = lds + 65536 + boff1; LAS const char* la0 = lds + aoff0; LAS const char* la1 = lds + aoff1;
    asm volatile("" : "+v"(lb0), "+v"(lb1), "+v"(la0), "+v"(la1));
#define LDB(dst, b, h) do { _Pragma("unroll") for (int k = 0; k < 2; ++k) { \
        dst[0][k] = tr_frag(lb0 + (SB(b, h) - 65536 + k * 8192), 1024); dst[1][k] = tr_frag(lb1 + (SB(b, h) - 65536 + k * 8192), 1024); } } while (0)
#define MMA(ai, bj, At, Bt) do { __builtin_amdgcn_s_setprio(0); _Pragma("unroll") for (int m = 0; m < 4; ++m) _Pragma("unroll") for (int n = 0; n < 2; ++n) _Pragma("unroll") for (int k = 0; k < 2; ++k) \
        { acc[ai][bj][m][n] = __builtin_amdgcn_mfma_f32_16x16x32_bf16(Bt[n][k], At[m][k], acc[ai][bj][m][n], 0, 0, 0); SCHED(); } __builtin_amdgcn_s_setprio(2); } while (0)
    auto csum = [](f32x4 (&cs)[2], const bf16x8 (&Bt)[2][2]) {
        u32x4 o4 = {0x3f803f80u, 0x3f803f80u, 0x3f803f80u, 0x3f803f80u}; asm volatile("" : "+v"(o4));
#pragma unroll
        for (int k = 0; k < 2; ++k)
#pragma unroll
            for (int n = 0; n < 2; ++n) cs[n] = __builtin_amdgcn_mfma_f32_16x16x32_bf16(Bt[n][k], __builtin_bit_cast(bf16x8, o4), cs[n], 0, 0, 0);
    };
    char* ws = p->ws;
    P0Regs pr; p0_load<false>(p, pr, b, wid, lane);
    if (b == 0 && wid == 0) {
        u32x4 t4[4];
#pragma unroll
        for (int i = 0; i < 4; ++i) t4[i] = *(const u32x4*)((const char*)p->hk + i * 1024 + lane * 16);
#pragma unroll
        for (int i = 0; i < 4; ++i) *(u32x4*)(ws + WS_HK0 + i * 1024 + lane * 16) = t4[i];
    }
    STAGE_B(0, 0, 0); STAGE_B(0, 1, 0); STAGE_A(0, 0, 0); STAGE_A(0, 1, 0);
    f32x4 acc[2][2][4][2];
#pragma unroll
    for (int a = 0; a < 2; ++a)
#pragma unroll
        for (int c = 0; c < 2; ++c) {
#pragma unroll
            for (int m = 0; m < 4; ++m)
#pragma unroll
                for (int n = 0; n < 2; ++n) acc[a][c][m][n] = (f32x4){0.f, 0.f, 0.f, 0.f};
            asm volatile("" : "+v"(acc[a][c][0][0]), "+v"(acc[a][c][0][1]), "+v"(acc[a][c][1][0]), "+v"(acc[a][c][1][1]), "+v"(acc[a][c][2][0]), "+v"(acc[a][c][2][1]), "+v"(acc[a][c][3][0]), "+v"(acc[a][c][3][1]));
        }
    bf16x8 At[4][2], B0[2][2], B1[2][2];
    p0_wait<8>(pr);
    STAGE_B(1, 0, 1); STAGE_A(1, 0, 1); STAGE_B(1, 1, 1);
    const unsigned tag = __builtin_amdgcn_readfirstlane(pr.ep) + 1u;
    p0_kv(ws, pr, lds + 131072 + wid * 3072, lds + 49152 + wid * 2048, b, wid, lane);
    unsigned tagv = tag, zero = 0u, voffW = (unsigned)(lane & 7) * 128u;
    const char* wT = (const char*)p->wd + (size_t)(b * 8 + wid) * 4096;
    const char* hT = (const char*)(p->hk + (size_t)coop_channel(b) * SEQ) + (wid & 3) * 1024;
    char* fkv = ws + WS_FKV + (size_t)((b & 7) * 256 + (b >> 3) * 8 + wid) * 4;
    asm volatile("" : "+v"(tagv), "+v"(zero), "+v"(voffW), "+s"(wT), "+s"(hT), "+s"(fkv));
    WAIT_V(8);
    BAR();
    __builtin_amdgcn_s_setprio(2);
    if (wr == 1) BAR();
#define SP1(b, STG, WV, CS) do { LDB(B0, b, 0); LDB(B1, b, 1); SCHED(); LDA(At, b, 0); STG; WV; WAIT_L(0); BAR(); MMA(0, 0, At, B0); MMA(0, 1, At, B1); CS; BAR(); SCHED(); } while (0)
#define SP2(b, STG, WV) do { LDA(At, b, 1); STG; WV; WAIT_L(0); BAR(); MMA(1, 0, At, B0); MMA(1, 1, At, B1); BAR(); SCHED(); } while (0)
#define SP2H(b, STG, WV, COND, HOOK) do { LDA(At, b, 1); STG; WV; WAIT_L(0); BAR(); MMA(1, 0, At, B0); MMA(1, 1, At, B1); SCHED(); \
        if (COND) { if (wr == 0) BAR(); HOOK; SCHED(); if (wr == 1) BAR(); } else BAR(); SCHED(); } while (0)
#define TRIP3(t, CS, XH) do { \
        SP1(0, STAGE_A(1, 1, (t) + 1), { if (t) WAIT_V(8); XH; }, CS); \
        SP2(0, { STAGE_B(0, 0, (t) + 2); STAGE_B(0, 1, (t) + 2); STAGE_A(0, 0, (t) + 2); }, { WAIT_V(8); if ((t) == 0) flag_set_1(zero, tagv, fkv); }); \
        SP1(1, STAGE_A(0, 1, (t) + 2), WAIT_V(8), CS); \
    } while (0)
#define STG4(t) { STAGE_B(1, 0, (t) + 3); STAGE_B(1, 1, (t) + 3); STAGE_A(1, 0, (t) + 3); }
#define WV4(t) { WAIT_V(8); \
        if ((t) >= 4 && (t) < 12) touch_asm(voffW, wT + ((t) - 4) * 512, lds0 + 163584); \
        else if ((t) == 12) { touch_asm(voffW, hT, lds0 + 163584); touch_asm(0u, ws + WS_FH + (fresh_s(pm) * 28 + fresh_s(pn)) * FL, lds0 + 163584); } }
    auto cs_hook = [&](f32x4 c0, f32x4 c1) {
        const int ln = fresh(lane), er = ln & 7;
        const float a0 = (er & 1) ? c0[1] : c0[0], a1 = (er & 1) ? c0[3] : c0[2], b0 = (er & 1) ? c1[1] : c1[0], b1 = (er & 1) ? c1[3] : c1[2];
        const float ca = (er & 2) ? a1 : a0, cb = (er & 2) ? b1 : b0, cv = (er & 4) ? cb : ca;
        gstore2_sc1_half((unsigned)(wr * 128 + wc * 32 + (er >> 2) * 16 + 4 * (ln >> 4) + (er & 3)) * 8u, ws + WS_CS + cs_off(fresh_s(pn)) + pm * 2048, (u32x2){__float_as_uint(cv), tag});
    };
    int t = 0;
#pragma unroll 1
    for (int seg = 0; seg < 2; ++seg) {
        const int tend = seg ? NT - 2 : 2 * (pm & 3);
#pragma unroll 1
        for (; t < tend; t += 2) {
            TRIP3(t, {}, {});
            SP2(1, STG4(t), WV4(t));
        }
        if (seg == 0) {
            f32x4 cs[2] = {{0.f, 0.f, 0.f, 0.f}, {0.f, 0.f, 0.f, 0.f}};
            auto xrow = [&]() {
                const u16* xr = fresh(p)->x + (size_t)(fresh_s(b) * 8 + wid) * DM; const unsigned v = (unsigned)fresh(lane) * 16u;
                glds_asm(v, xr, lds0 + 139264 + wid * 3072); glds_asm(v, xr + 512, lds0 + 139264 + wid * 3072 + 1024);
            };
            TRIP3(t, { if (wr) csum(cs, B1); else csum(cs, B0); }, xrow());
            const int ln = fresh(lane);
            const u32x4 xa = *(const volatile LAS u32x4*)(lds + 139264 + wid * 3072 + ln * 16), xb = *(const volatile LAS u32x4*)(lds + 139264 + wid * 3072 + 1024 + ln * 16);
            LDA(At, 1, 1); STG4(t); WV4(t); WAIT_L(0); BAR();
            __builtin_amdgcn_s_setprio(0); SCHED();
#pragma unroll
            for (int bj = 0; bj < 2; ++bj)
#pragma unroll
                for (int m = 0; m < 4; ++m)
#pragma unroll
                    for (int n = 0; n < 2; ++n)
#pragma unroll
                        for (int k = 0; k < 2; ++k) acc[1][bj][m][n] = __builtin_amdgcn_mfma_f32_16x16x32_bf16(bj ? B1[n][k] : B0[n][k], At[m][k], acc[1][bj][m][n], 0, 0, 0);
            float rc, r1, r2; row_sums(xa, xb, rc, r1, r2);
#pragma unroll
            for (int i = 0; i < 32; ++i) { __builtin_amdgcn_sched_group_barrier(0x8, 1, 0); __builtin_amdgcn_sched_group_barrier(0x2, 2, 0); }
            SCHED(); __builtin_amdgcn_s_setprio(2);
            if (wr == 0) BAR();
            cs_hook(cs[0], cs[1]); row_publish(ws, fresh_s(b) * 8 + wid, rc, r1, r2, tag); SCHED();
            if (wr == 1) BAR();
            SCHED();
            t += 2;
        }
    }
#undef TRIP3
#undef STG4
#undef WV4
    SP1(0, { STAGE_A(1, 1, NT - 1);
             const char* cg = ws + WS_CS + cs_off(fresh_s(pn)) + wid * 2048; const unsigned v = (unsigned)fresh(lane) * 16u;
             glds_asm(v, cg, lds0 + 131072 + wid * 2048); glds_asm(v, cg + 1024, lds0 + 131072 + wid * 2048 + 1024);
             if (wid < 2) glds_asm(v, ws + WS_STATS + (size_t)fresh_s(pm) * 2048 + wid * 1024, lds0 + 147456 + wid * 1024); }, WAIT_V(8), {});
    p = fresh(p);
    const char* wB = g2_baseB(p, g2_pos(b)); unsigned wv = g2_voffB(wid, fresh(lane)); asm volatile("" : "+v"(wv));
    auto table_hook = [&]() {
        int tz = threadIdx.x; asm volatile("" : "+v"(tz));
        if (wr == 0) {
            float s = 0.f; unsigned bad = 0;
#pragma unroll
            for (int i = 0; i < 8; ++i) { const u32x2 g = *(const LAS u32x2*)(lds + 131072 + i * 2048 + tz * 8); s += __uint_as_float(g.x); bad |= g.y ^ tag; }
            if (bad) {
                s = 0.f;
#pragma unroll 1
                for (int i = 0; i < 8; ++i) s += __uint_as_float(granule_wait(p->ws, WS_CS + cs_off(pn) + i * 2048 + tz * 8, tag));
            }
            *(LAS float*)(lds + 151552 + tz * 4) = s;
        } else {
            const u32x2 g = *(const LAS u32x2*)(lds + 147456 + (tz - 256) * 8);
            const unsigned v = g.y == tag ? g.x : granule_wait(p->ws, WS_STATS + ((size_t)pm * 256 + tz - 256) * 8, tag);
            *(LAS f32x2*)(lds + 152576 + (tz - 256) * 8) = (f32x2){bf_lo(v), __builtin_amdgcn_rcpf(bf_hi(v))};
        }
    };
    u32x4 kvf, ur = {0u, 0u, 0u, 0u};
    SP2H(0, { g2_fill(wB, wv, ldsw, 0); gload_sc1(kvf, (unsigned)fresh(lane) * 16u, ws + WS_FKV + (size_t)(b >> 5) * 1024); }, WAIT_V(3), true, { table_hook(); WAIT_L(0); });
    SP1(1, g2_fill(wB, wv, ldsw, 1), asm volatile("s_waitcnt vmcnt(2)" : "+v"(kvf) :: "memory"), {});
    SP2(1, g2_fill(wB, wv, ldsw, 2), {});
    if (wr == 0) BAR();
    g2_fill(wB, wv, ldsw, 3);
    if (wr == 1) {
        const int ln = fresh(lane), d = coop_channel(b); const char* hr = conv_row(p, d);
#pragma unroll
        for (int j = 0; j < 4; ++j) conv_fetch_h(hr, lds0 + CO + 4096, ln, wc * 4 + j);
        if (!__all(kvf.x == tag && kvf.y == tag && kvf.z == tag && kvf.w == tag)) kv_wait(ws, b >> 5, ln, tag);
        gload_sc1(ur, (unsigned)ln * (unsigned)KVS, ws + WS_KVT + (size_t)d * 16 + (size_t)wc * 64 * KVS);
    }
    {
        int tz = threadIdx.x; asm volatile("" : "+v"(tz));
        const int l2 = tz & 63, er = l2 & 15, eq = l2 >> 4, cm = ((eq & 1) << 1) | (eq >> 1);
        f32x4 cg[2], cu[2];
#pragma unroll
        for (int n = 0; n < 2; ++n) { cg[n] = *(const LAS f32x4*)(lds + 151552 + (wc * 32 + n * 16 + 4 * eq) * 4); cu[n] = *(const LAS f32x4*)(lds + 151552 + (128 + wc * 32 + n * 16 + 4 * eq) * 4); }
        char* hp = p->ws + WS_H + (size_t)(pm * 16 + wr * 4) * HRB + (pn * 4 + wc) * 1024;
        const unsigned hv = (unsigned)(cm * 16 + er) * 16u;
        f32x2 st[8];
#pragma unroll
        for (int i = 0; i < 8; ++i) st[i] = *(const LAS f32x2*)(lds + 152576 + ((i >> 2) * 128 + wr * 64 + (i & 3) * 16 + er) * 8);
#pragma unroll
        for (int ai = 0; ai < 2; ++ai)
#pragma unroll
            for (int m = 0; m < 4; ++m) {
                const float mu = st[ai * 4 + m].x, ce = st[ai * 4 + m].y * -1.4426950408889634f, kk = -1.4426950408889634f * __builtin_amdgcn_rcpf(st[ai * 4 + m].y);
                unsigned w[2][2];
#pragma unroll
                for (int n = 0; n < 2; ++n)
#pragma unroll
                    for (int j = 0; j < 4; j += 2) {
                        const f32x2 ag = {acc[ai][0][m][n][j], acc[ai][0][m][n][j + 1]}, au = {acc[ai][1][m][n][j], acc[ai][1][m][n][j + 1]};
                        const f32x2 a = (ag - (f32x2){cg[n][j], cg[n][j + 1]} * mu) * ce, u = au - (f32x2){cu[n][j], cu[n][j + 1]} * mu;
                        const f32x2 e = {__builtin_amdgcn_exp2f(a.x), __builtin_amdgcn_exp2f(a.y)};
                        const f32x2 d = e * kk + kk;
                        const f32x2 o = (a * u) * (f32x2){__builtin_amdgcn_rcpf(d.x), __builtin_amdgcn_rcpf(d.y)};
                        w[n][j >> 1] = pk_bf16(o.x, o.y);
                    }
                const auto r0 = __builtin_amdgcn_permlane16_swap(w[0][0], w[1][0], false, false);
                const auto r1 = __builtin_amdgcn_permlane16_swap(w[0][1], w[1][1], false, false);
                gstore_sc1_free(hv, hp + (size_t)(ai * 8 + m) * HRB, (u32x4){r0[0], r1[0], r0[1], r1[1]});
            }
    }
    asm volatile("s_waitcnt vmcnt(0)" : "+v"(ur) :: "memory");
    if (wr == 1) *(LAS u32x4*)(lds + CO + wc * 1024 + (fresh((int)threadIdx.x) & 63) * 16) = ur;
    WAIT_L(0); BAR();
    if (tid == 0) flag_set(p->ws, WS_FH + (pm * 28 + pn) * FL, tag);
    return tag;
#undef SA
#undef SB
#undef STAGE_A
#undef STAGE_B
#undef LDA
#undef LDB
#undef MMA
#undef SP1
#undef SP2
#undef SP2H
}

__device__ __forceinline__ unsigned conv_body(KP p, LAS char* lds_all, int b) {
    const int tid = threadIdx.x, wid = __builtin_amdgcn_readfirstlane(tid >> 6), lane = tid & 63, fr = lane & 15, fq = lane >> 4;
    const int grp = b - 224, ch0 = grp * 32 + 24 + wid;
    LAS char* lu = lds_all + wid * 20480;
    LAS char* lh = lu + 4096;
    const unsigned lua = (unsigned)(size_t)lu;
    const int cpy = (fr + 1) & 3;
    const int alc = 2 * (fr - 15 + 8 * fq - cpy + (cpy ? 4 : 0));
    unsigned mk[4];
#pragma unroll
    for (int i = 0; i < 4; ++i) { const int e = fr - 15 + 8 * fq + 2 * i; mk[i] = (e >= 0 ? 0xffffu : 0u) | (e + 1 >= 0 ? 0xffff0000u : 0u); }
    LAS const char* lhc = lh + cpy * 4096; LAS const char* bb = lu + 16 * (254 - 224 - 2 * fr + fq);
    const unsigned vu = (unsigned)lane * (unsigned)KVS;
    unsigned vh[4];
#pragma unroll
    for (int c = 0; c < 4; ++c) { vh[c] = (unsigned)(lane ^ (4 * c)) * 16u + 2u * c; asm volatile("" : "+v"(vh[c])); }
    auto fetch_h = [&](int d) {
        const char* hr = (const char*)(p->hk + (size_t)d * SEQ);
#pragma unroll
        for (int j = 0; j < 16; ++j) { const int i = j >> 2, c = ((j & 1) << 1) | ((j >> 1) & 1); glds_asm(vh[c], hr + i * 1024 - (c ? 8 : 0), lua + 4096 + c * 4096 + i * 1024); }
    };
    u32x4 ur[4];
    auto fetch_u = [&](int d) {
#pragma unroll
        for (int i = 0; i < 4; ++i) gload_sc1(ur[i], vu, p->ws + WS_KVT + (size_t)d * 16 + (size_t)i * 64 * KVS);
    };
    unsigned dall;
    asm volatile("global_load_ushort %0, %1, %2" : "=v"(dall) : "v"((unsigned)(lane & 3) * 16u), "s"(p->dvec + ch0 - 24) : "memory");
    unsigned tag;
    {
        touch_asm((unsigned)((lane >> 5) * 8 * SEQ * 2 + (lane & 31) * 128), p->hk + (size_t)(ch0 - 8) * SEQ, (unsigned)(size_t)lds_all + 163584);
        touch_asm((unsigned)((lane < 32 ? ch0 - 16 : coop_channel(b)) * SEQ * 2 + (lane & 31) * 128), p->hk, (unsigned)(size_t)lds_all + 163584);
        P0Regs pr; p0_load<true>(p, pr, b, wid, lane);
        p0_wait<0>(pr);
        tag = __builtin_amdgcn_readfirstlane(pr.ep) + 1u;
        p0_kv(p->ws, pr, lds_all + 131072 + wid * 3072, lds_all + 49152 + wid * 2048, b, wid, lane);
        float rc, r1, r2; row_sums(pr.xa, pr.xb, rc, r1, r2); row_publish(p->ws, b * 8 + wid, rc, r1, r2, tag);
    }
    asm volatile("s_waitcnt vmcnt(0)" : "+v"(dall) :: "memory");
    flag_set_lane0(p->ws, WS_FKV + (size_t)((b & 7) * 256 + (b >> 3) * 8 + wid) * 4, tag);
    BAR();
    if (wid < 4) fetch_h(ch0);
    if (wid >= 6) kv_wait(p->ws, wid == 6 ? grp >> 2 : 7, lane, tag);
    BAR();
    __builtin_amdgcn_s_setprio(1);
    fetch_u(ch0);
    if (wid >= 4) fetch_h(ch0);
#pragma unroll 1
    for (int c = 0; c < 3; ++c) {
        const int d = ch0 - 8 * c;
        asm volatile("s_waitcnt vmcnt(0)" : "+v"(ur[0]), "+v"(ur[1]), "+v"(ur[2]), "+v"(ur[3]) :: "memory");
#pragma unroll
        for (int i = 0; i < 4; ++i) *(volatile LAS u32x4*)(lu + i * 1024 + lane * 16) = ur[i];
        f32x4 acc[8];
#pragma unroll
        for (int t = 0; t < 8; ++t) acc[t] = (f32x4){0.f, 0.f, 0.f, 0.f};
        bf16x8 A[2][8], B[2][8];
#define CV_LDB(r, i) B[(r) & 1][i] = *(const volatile LAS bf16x8*)(bb + 64 * (r) + 3584 - 512 * (i))
#define CV_LDA(r, i) do { const bf16x4 lo = *(const volatile LAS bf16x4*)(a0 + 512 * (i)), hi = *(const volatile LAS bf16x4*)(a1 + 512 * (i)); \
            A[(r) & 1][i] = __builtin_shufflevector(lo, hi, 0, 1, 2, 3, 4, 5, 6, 7); } while (0)
#define CV_ITEM(r, k) do { if ((k) == 0) CV_LDA(r, 0); else if ((k) < 8) CV_LDB(r, k); else if ((k) == 8) CV_LDB(r, 0); else CV_LDA(r, (k) - 8); } while (0)
        {
            LAS const char* a0 = lhc + (alc ^ (cpy << 6)); LAS const char* a1 = lhc + ((alc + 8) ^ (cpy << 6));
#pragma unroll
            for (int k = 0; k < 16; ++k) CV_ITEM(0, k);
            u32x4 t = __builtin_bit_cast(u32x4, A[0][0]); t.x &= mk[0]; t.y &= mk[1]; t.z &= mk[2]; t.w &= mk[3]; A[0][0] = __builtin_bit_cast(bf16x8, t);
        }
        __builtin_amdgcn_s_setprio(0);
#pragma unroll
        for (int r = 0; r < 8; ++r) {
            if (r == 1) __builtin_amdgcn_s_setprio(2);
            SCHED();
            LAS const char* a0 = lhc + ((64 * (r + 1) + alc) ^ (cpy << 6)); LAS const char* a1 = lhc + ((64 * (r + 1) + alc + 8) ^ (cpy << 6));
            int cnt = 0;
#pragma unroll
            for (int i = 0; i < 8; ++i) {
#pragma unroll
                for (int mm = 0; mm + i < 8; ++mm) {
                    const int m = (mm + 1 + i == 8) ? 0 : mm + 1;
                    if (i == 0 && m == 0) { if (2 * fr + 1 - fq - 4 * r < 0) B[r & 1][0] = (bf16x8){0, 0, 0, 0, 0, 0, 0, 0}; }
                    acc[i + m] = __builtin_amdgcn_mfma_f32_16x16x32_bf16(A[r & 1][i], B[r & 1][m], acc[i + m], 0, 0, 0);
                    if ((cnt & 1) == 1) { if (r < 7 && cnt < 32) CV_ITEM(r + 1, cnt >> 1); SCHED(); }
                    ++cnt;
                }
            }
        }
#undef CV_LDA
#undef CV_LDB
#undef CV_ITEM
        __builtin_amdgcn_s_setprio(1);
        u32x2 uu[8];
#pragma unroll
        for (int t = 0; t < 8; ++t) uu[t] = *(const volatile LAS u32x2*)(lu + 2 * (2044 - (256 * t + 16 * fr + 4 * fq)));
        asm volatile("s_waitcnt lgkmcnt(0)" : "+v"(uu[0]), "+v"(uu[1]), "+v"(uu[2]), "+v"(uu[3]), "+v"(uu[4]), "+v"(uu[5]), "+v"(uu[6]), "+v"(uu[7]) :: "memory");
        if (c < 2 || wid == 3) { const int dn = c < 2 ? d - 8 : coop_channel(b); fetch_u(dn); fetch_h(dn); }
        const float dd = __uint_as_float((unsigned)__builtin_amdgcn_readlane((int)dall, 3 - c) << 16);
        char* zp = p->ws + WS_ZT + (size_t)d * SEQ * 2;
        const unsigned zv = (unsigned)(16 * fr + 4 * fq) * 2u;
#pragma unroll
        for (int t = 0; t < 8; ++t) {
            const float z0 = fma_s(bf_hi(uu[t].y), dd, acc[t][0]), z1 = fma_s(bf_lo(uu[t].y), dd, acc[t][1]), z2 = fma_s(bf_hi(uu[t].x), dd, acc[t][2]), z3 = fma_s(bf_lo(uu[t].x), dd, acc[t][3]);
            gstore2_sc1(zv, zp + 512 * t, (u32x2){pk_bf16(z0, z1), pk_bf16(z2, z3)});
        }
    }
    __builtin_amdgcn_s_setprio(0);
    asm volatile("s_waitcnt vmcnt(0)" : "+v"(ur[0]), "+v"(ur[1]), "+v"(ur[2]), "+v"(ur[3]) :: "memory");
    if (wid == 3) {
#pragma unroll
        for (int i = 0; i < 4; ++i) *(volatile LAS u32x4*)(lu + i * 1024 + lane * 16) = ur[i];
    }
    WAIT_L(0); BAR();
    if (tid == 0) flag_set(p->ws, WS_FZ + grp * FL, tag);
    {
        const char* wB = g2_baseB(p, g2_pos(b)); const unsigned wv = g2_voffB(wid, lane);
#pragma unroll
        for (int t = 0; t < 3; ++t) g2_fill(wB, wv, (unsigned)(size_t)lds_all + wid * 1024, t);
    }
    return tag;
}

__device__ __forceinline__ void coop_conv(LAS char* opd, LAS char* scr) {
    const int tid = fresh((int)threadIdx.x), wid = __builtin_amdgcn_readfirstlane(tid >> 6), lane = tid & 63, fr = lane & 15, fq = lane >> 4;
    const int cpy = (fr + 1) & 3, alc = 2 * (fr - 15 + 8 * fq - cpy + (cpy ? 4 : 0)) + 64 * wid;
    LAS const char* lhc = opd + 4096 + cpy * 4096;
    LAS const char* a0 = lhc + (alc ^ (cpy << 6)); LAS const char* a1 = lhc + ((alc + 8) ^ (cpy << 6)); LAS const char* bb = opd + 16 * (254 - 224 - 2 * fr + fq) + 64 * wid;
    bf16x8 A[8], B[8];
#pragma unroll
    for (int i = 0; i < 8; ++i) {
        B[i] = *(const volatile LAS bf16x8*)(bb + 3584 - 512 * i);
        const bf16x4 lo = *(const volatile LAS bf16x4*)(a0 + 512 * i), hi = *(const volatile LAS bf16x4*)(a1 + 512 * i);
        A[i] = __builtin_shufflevector(lo, hi, 0, 1, 2, 3, 4, 5, 6, 7);
    }
    {
        u32x4 t = __builtin_bit_cast(u32x4, A[0]);
#pragma unroll
        for (int i = 0; i < 4; ++i) { const int e = fr - 15 + 8 * fq + 2 * i + 32 * wid; t[i] &= (e >= 0 ? 0xffffu : 0u) | (e + 1 >= 0 ? 0xffff0000u : 0u); }
        A[0] = __builtin_bit_cast(bf16x8, t);
    }
    if (2 * fr + 1 - fq - 4 * wid < 0) B[0] = (bf16x8){0, 0, 0, 0, 0, 0, 0, 0};
    f32x4 acc[8];
#pragma unroll
    for (int t = 0; t < 8; ++t) acc[t] = (f32x4){0.f, 0.f, 0.f, 0.f};
#pragma unroll
    for (int i = 0; i < 8; ++i)
#pragma unroll
        for (int m = 0; m + i < 8; ++m) acc[i + m] = __builtin_amdgcn_mfma_f32_16x16x32_bf16(A[i], B[m], acc[i + m], 0, 0, 0);
#pragma unroll
    for (int t = 0; t < 8; ++t) *(LAS f32x4*)(scr + (t * 8 + wid) * 1024 + lane * 16) = acc[t];
}
__device__ __forceinline__ void coop_finish(KP p, LAS const char* opd, LAS const char* scr, int b, int wid, int lane) {
    const int d = coop_channel(b), fr = lane & 15, fq = lane >> 4;
    const unsigned dw = *(const __attribute__((address_space(4))) unsigned*)(size_t)(p->dvec + (d & ~1));
    f32x4 y = *(const LAS f32x4*)(scr + wid * 8192 + lane * 16);
#pragma unroll
    for (int r = 1; r < 8; ++r) y += *(const LAS f32x4*)(scr + (wid * 8 + r) * 1024 + lane * 16);
    const u32x2 uu = *(const LAS u32x2*)(opd + 2 * (2044 - (256 * wid + 16 * fr + 4 * fq)));
    const float dd = (d & 1) ? bf_hi(dw) : bf_lo(dw);
    gstore2_sc1((unsigned)(256 * wid + 16 * fr + 4 * fq) * 2u, p->ws + WS_ZT + (size_t)d * SEQ * 2,
                (u32x2){pk_bf16(y[0] + bf_hi(uu.y) * dd, y[1] + bf_lo(uu.y) * dd), pk_bf16(y[2] + bf_hi(uu.x) * dd, y[3] + bf_lo(uu.x) * dd)});
}

template <int N> __device__ __forceinline__ void wait_v4(u32x4 (&a)[4]) { asm volatile("s_waitcnt vmcnt(%4)" : "+v"(a[0]), "+v"(a[1]), "+v"(a[2]), "+v"(a[3]) : "n"(N) : "memory"); }

__device__ __forceinline__ void gemm2_body(KP p, LAS char* lds, int b, unsigned tag) {
    int tid = threadIdx.x; asm volatile("" : "+v"(tid));
    const int wid = __builtin_amdgcn_readfirstlane(tid >> 6), lane = tid & 63, wm = wid & 3, kp = wid >> 2, fr = lane & 15, fq = lane >> 4;
    const unsigned lds0 = (unsigned)(size_t)lds;
    const G2Pos g = g2_pos(b);
    unsigned voffB = g2_voffB(wid, lane), voffA0 = (unsigned)lane * 16u, voffA1 = voffA0 + (unsigned)HRB;
    const char* cA = p->ws + WS_H + (size_t)(g.tm * 8 + wm * 2) * HRB + (g.kh * (FF / 64) + kp) * 1024;
    const char* cB = g2_baseB(p, g);
    const unsigned ldsw = lds0 + (unsigned)wid * 1024u;
    const int tq = (lane & 15) >> 2, tp = lane & 3;
    const int r0 = g.tm * 128 + g.kh * 64, c0 = g.tn * 128;
    const char* fh = p->ws + WS_FH + (size_t)((g.tm >> 1) * 28 + g.kh * 14) * FL;
    const unsigned vf = (unsigned)(lane < 14 ? lane : 0) * (unsigned)FL;
    unsigned ready = 0;
    auto need = [&](unsigned bits) {
        if ((ready & bits) != bits) {
            const unsigned long long t0 = __builtin_amdgcn_s_memrealtime();
            for (;;) {
                unsigned f = 0; asm volatile("s_mov_b64 exec, 0x3fff\n\tglobal_load_dword %0, %1, %2 sc1\n\ts_mov_b64 exec, -1\n\ts_waitcnt vmcnt(0)" : "+v"(f) : "v"(vf), "s"(fh) : "memory");
                ready = (unsigned)__ballot(f == tag);
                if ((ready & bits) == bits) break;
                if (__builtin_amdgcn_s_memrealtime() - t0 > 2000000ull) { flag_set(p->ws, WS_TMO, tag); ready = ~0u; break; }
                __builtin_amdgcn_s_sleep(2);
            }
        }
    };
    u32x4 ar[3][4];
    LAS char* lz = lds + 131072; LAS char* lx = lz + 16384;
    f32x4 acc[2][8];
#pragma unroll
    for (int m = 0; m < 2; ++m) {
#pragma unroll
        for (int n = 0; n < 8; ++n) acc[m][n] = (f32x4){0.f, 0.f, 0.f, 0.f};
        asm volatile("" : "+v"(acc[m][0]), "+v"(acc[m][1]), "+v"(acc[m][2]), "+v"(acc[m][3]), "+v"(acc[m][4]), "+v"(acc[m][5]), "+v"(acc[m][6]), "+v"(acc[m][7]));
    }
    bf16x8 bt[2][8];
    LAS const char* lb[2][8];
#pragma unroll
    for (int n = 0; n < 8; ++n) {
        const int bo = (32 * kp + 8 * fq + tq) * 256 + (((2 * (n & 3) + (tp >> 1)) ^ ((tq << 1) | ((fq & 1) << 3))) * 16) + 8 * (tp & 1);
        lb[0][n] = lds + (bo ^ ((n >> 2) * 128)); lb[1][n] = lb[0][n] + 65536; asm volatile("" : "+v"(lb[0][n]), "+v"(lb[1][n]));
    }
    asm volatile("" : "+v"(voffA0), "+v"(voffA1), "+v"(voffB));
    WAIT_V(0);
    if (wid == 0) { need(3u); *(volatile LAS unsigned*)(lds + 163836) = ready; }
    WAIT_L(0); BAR();
#define G2_LDA(set, P) do { const char* _a = cA + (size_t)(P) * 4096; gload_sc1(ar[set][0], voffA0, _a); gload_sc1(ar[set][1], voffA1, _a); \
        gload_sc1(ar[set][2], voffA0, _a + 2048); gload_sc1(ar[set][3], voffA1, _a + 2048); } while (0)
    G2_LDA(0, 0); G2_LDA(1, 1);
    ready = __builtin_amdgcn_readfirstlane(*(volatile LAS unsigned*)(lds + 163836));
    coop_finish(p, lds + (b < 224 ? CO : 61440), lds + (b < 224 ? 65536 : 81920), b, wid, lane);
    BAR();
    if (b >= 224) g2_fill(cB, voffB, ldsw, 3);
    g2_fill(cB, voffB, ldsw, 4); g2_fill(cB, voffB, ldsw, 5);
    __builtin_amdgcn_s_setprio(0);
    if (kp == 1) BAR();
#define G2_RD(P, s, n) bt[s][n] = tr_frag(lb[((P) >> 1) & 1][n] + ((P) & 1) * 32768 + (s) * 16384, 1024)
#define G2_PHASE(set, P, DOB, DOA, W, HOOK) do { \
        const char* _a = cA + (size_t)((P) + 2) * 4096; const char* _b = cB + (size_t)(2 * (P) + 6) * G2_BSTEP; const unsigned _l = ldsw + (((P) + 3) & 3) * 32768; \
        if (DOA) need(1u << ((P) + 2)); \
        _Pragma("unroll") for (int i = 0; i < 4; ++i) { \
            if (DOA) gload_sc1(ar[((set) + 2) % 3][i], (i & 1) ? voffA1 : voffA0, _a + (i >> 1) * 2048); \
            G2_RD(P, 0, 2 * i); G2_RD(P, 0, 2 * i + 1); \
        } \
        HOOK; \
        wait_v4<W>(ar[set]); WAIT_L(0); BAR(); \
        __builtin_amdgcn_s_setprio(1); \
        _Pragma("unroll") for (int n = 0; n < 8; ++n) { \
            acc[0][n] = __builtin_amdgcn_mfma_f32_16x16x32_bf16(bt[0][n], __builtin_bit_cast(bf16x8, ar[set][0]), acc[0][n], 0, 0, 0); SCHED(); \
            G2_RD(P, 1, n); SCHED(); \
            acc[1][n] = __builtin_amdgcn_mfma_f32_16x16x32_bf16(bt[0][n], __builtin_bit_cast(bf16x8, ar[set][1]), acc[1][n], 0, 0, 0); SCHED(); \
        } \
        _Pragma("unroll") for (int n = 0; n < 8; ++n) { \
            acc[0][n] = __builtin_amdgcn_mfma_f32_16x16x32_bf16(bt[1][n], __builtin_bit_cast(bf16x8, ar[set][2]), acc[0][n], 0, 0, 0); \
            acc[1][n] = __builtin_amdgcn_mfma_f32_16x16x32_bf16(bt[1][n], __builtin_bit_cast(bf16x8, ar[set][3]), acc[1][n], 0, 0, 0); SCHED(); \
            if ((n & 1) == 0 && DOB) { glds_asm(voffB, _b + (n >> 1) * (G2_BSTEP / 2), _l + (n >> 1) * 8192); SCHED(); } \
        } \
        __builtin_amdgcn_s_setprio(0); \
        BAR(); SCHED(); } while (0)
    G2_PHASE(0, 0, 1, 1, 13, { if (b >= 224) WAIT_V(8); });
    G2_PHASE(1, 1, 1, 1, 12, {});
    G2_PHASE(2, 2, 1, 1, 13, { flag_set_lane0(p->ws, WS_FZ2 + (size_t)(wid * 256 + b) * 4, tag); });
    const int slot = (wm & 1) * 2 + kp;
    G2_PHASE(0, 3, 1, 1, 12, { touch_asm(0u, p->ws + WS_FX + ((size_t)b * 4 + slot) * FL, lds0 + 131072); });
    G2_PHASE(1, 4, 1, 1, 12, {});
    G2_PHASE(2, 5, 1, 1, 12, {
        const int ln = fresh(lane);
        const unsigned xoff = (unsigned)((wid * 4 + (ln >> 4)) * DM + (((ln & 15) ^ ((wid * 4 + (ln >> 4)) & 15)) * 8)) * 2u;
        const char* xg = (const char*)p->x + ((size_t)r0 * DM + c0) * 2;
        glds_asm(xoff, xg, ldsw + 147456); glds_asm(xoff, xg + (size_t)32 * DM * 2, ldsw + 147456 + 8192); });
    G2_PHASE(0, 6, 1, 1, 12, {});
    G2_PHASE(1, 7, 1, 1, 12, {});
    unsigned zf;
    const unsigned vz = lane < 32 ? (unsigned)(WS_FZ2 + ((r0 >> 8) * 256 + g.tn * 32 + lane) * 4) : (unsigned)(WS_FZ + (g.tn * 4 + (lane & 3)) * FL);
    G2_PHASE(2, 8, 1, 1, 13, { asm volatile("global_load_dword %0, %1, %2 sc1" : "=v"(zf) : "v"(vz), "s"(p->ws) : "memory"); });
    G2_PHASE(0, 9, 1, 1, 13, {});
    G2_PHASE(1, 10, 1, 1, 12, {});
    u32x4 zr[2]; u32x2 sg;
    G2_PHASE(2, 11, 0, 1, 12, {});
    G2_PHASE(0, 12, 0, 0, 7, {
        asm volatile("" : "+v"(zf));
        if (!__all(zf == tag)) flag_wait(p->ws, vz, true, tag);
        const unsigned zoff = (unsigned)((wid * 8 + (lane >> 3)) * SEQ + (lane & 7) * 8) * 2u;
        const char* zg = p->ws + WS_ZT + ((size_t)c0 * SEQ + r0) * 2;
        gload_sc1(zr[0], zoff, zg); gload_sc1(zr[1], zoff, zg + (size_t)64 * SEQ * 2);
        gload2_sc1(sg, (unsigned)fr * 8u, p->ws + WS_STATS + (size_t)(r0 + slot * 16) * 8); });
    G2_PHASE(1, 13, 0, 0, 0, {
        asm volatile("s_waitcnt vmcnt(0)" : "+v"(zr[0]), "+v"(zr[1]), "+v"(sg) :: "memory");
        const int zrow = wid * 8 + (lane >> 3);
        LAS char* zw = lz + zrow * 128 + (((lane & 7) ^ (zrow & 6)) * 16);
        *(LAS u32x4*)zw = zr[0]; *(LAS u32x4*)(zw + 8192) = zr[1]; });
#undef G2_PHASE
#undef G2_RD
#undef G2_LDA
    f32x4 sum[8];
    {
        LAS char* ex = lds + 65536 + (wid ^ 4) * 8192 + lane * 16;
        if (kp == 0) {
#pragma unroll
            for (int n = 0; n < 8; ++n) { *(LAS f32x4*)(ex + n * 1024) = acc[1][n]; sum[n] = acc[0][n]; }
            BAR();
        } else {
#pragma unroll
            for (int n = 0; n < 8; ++n) { *(LAS f32x4*)(ex + n * 1024) = acc[0][n]; sum[n] = acc[1][n]; }
        }
        WAIT_L(0); BAR();
#pragma unroll
        for (int n = 0; n < 8; ++n) sum[n] += *(const LAS f32x4*)(lds + 65536 + wid * 8192 + n * 1024 + lane * 16);
    }
    if ((wm >> 1) != g.kh) {
        char* xo = p->ws + WS_EX + ((size_t)b * 4 + slot) * 4096;
#pragma unroll
        for (int n = 0; n < 8; n += 2)
            gstore_sc1((unsigned)lane * 16u, xo + n * 512, (u32x4){pk_bf16(sum[n][0], sum[n][1]), pk_bf16(sum[n][2], sum[n][3]), pk_bf16(sum[n + 1][0], sum[n + 1][1]), pk_bf16(sum[n + 1][2], sum[n + 1][3])});
        WAIT_V(0);
        if (lane == 0) flag_set(p->ws, WS_FX + ((size_t)b * 4 + slot) * FL, tag);
    } else {
        const int xr = slot * 16 + fr;
        const unsigned sv = sg.y == tag ? sg.x : granule_wait(p->ws, WS_STATS + (size_t)(r0 + xr) * 8, tag);
        const float mu = bf_lo(sv), rs = __builtin_amdgcn_rcpf(bf_hi(sv));
#pragma unroll
        for (int n = 0; n < 8; ++n) {
            const int zrow = n * 16 + 4 * fq + tq, zc = 2 * slot + (tp >> 1);
            const u32x2 zw = __builtin_bit_cast(u32x2, __builtin_amdgcn_ds_read_tr16_b64_v4i16((LAS bf16x4*)(lz + zrow * 128 + ((zc ^ (zrow & 6)) * 16) + 8 * (tp & 1))));
            const u32x2 xv = *(const LAS u32x2*)(lx + xr * 256 + (((2 * n + (fq >> 1)) ^ (xr & 15)) * 16) + 8 * (fq & 1));
            const float x0 = bf_lo(xv.x), x1 = bf_hi(xv.x), x2 = bf_lo(xv.y), x3 = bf_hi(xv.y);
            sum[n][0] += x0 + rnd_bf(rnd_bf(x0 - mu) * rs) * bf_lo(zw.x); sum[n][1] += x1 + rnd_bf(rnd_bf(x1 - mu) * rs) * bf_hi(zw.x);
            sum[n][2] += x2 + rnd_bf(rnd_bf(x2 - mu) * rs) * bf_lo(zw.y); sum[n][3] += x3 + rnd_bf(rnd_bf(x3 - mu) * rs) * bf_hi(zw.y);
        }
        __builtin_amdgcn_s_sleep(12);
        flag_wait(p->ws, WS_FX + ((size_t)(b ^ 1) * 4 + slot) * FL, lane == 0, tag);
        u32x4 in[4];
#pragma unroll
        for (int n = 0; n < 4; ++n) gload_sc1(in[n], (unsigned)lane * 16u, p->ws + WS_EX + ((size_t)(b ^ 1) * 4 + slot) * 4096 + n * 1024);
        asm volatile("s_waitcnt vmcnt(0)" : "+v"(in[0]), "+v"(in[1]), "+v"(in[2]), "+v"(in[3]) :: "memory");
        u16* op = p->out + (size_t)(r0 + xr) * DM + c0 + (((fq & 1) << 1) | (fq >> 1)) * 8;
#pragma unroll
        for (int h = 0; h < 4; ++h) {
            const f32x4 u = sum[2 * h] + (f32x4){bf_lo(in[h].x), bf_hi(in[h].x), bf_lo(in[h].y), bf_hi(in[h].y)}, v = sum[2 * h + 1] + (f32x4){bf_lo(in[h].z), bf_hi(in[h].z), bf_lo(in[h].w), bf_hi(in[h].w)};
            const auto q0 = __builtin_amdgcn_permlane16_swap(pk_bf16(u[0], u[1]), pk_bf16(v[0], v[1]), false, false);
            const auto q1 = __builtin_amdgcn_permlane16_swap(pk_bf16(u[2], u[3]), pk_bf16(v[2], v[3]), false, false);
            __builtin_nontemporal_store((u32x4){q0[0], q1[0], q0[1], q1[1]}, (u32x4*)(op + h * 32));
        }
    }
}

__global__ __launch_bounds__(512, 2) void fused_kernel(Params args) {
    KP p = (KP)__builtin_amdgcn_kernarg_segment_ptr();
    extern __shared__ __attribute__((aligned(16))) char smem[];
    LAS char* lds = (LAS char*)smem;
    const int b = blockIdx.x;
    {
        const int wid = __builtin_amdgcn_readfirstlane(threadIdx.x >> 6);
        p0_stage(p, (unsigned)(size_t)lds + 131072 + wid * 3072, (unsigned)(size_t)lds + 49152 + wid * 2048, b, wid, threadIdx.x & 63);
    }
    unsigned tag;
    if (__builtin_expect(b < 224, 1)) tag = gemm1_body(p, lds, b); else tag = conv_body(p, lds, b);
    int b2 = blockIdx.x; asm volatile("" : "+s"(b2));
    coop_conv(lds + (b2 < 224 ? CO : 61440), lds + (b2 < 224 ? 65536 : 81920));
    gemm2_body(fresh(p), lds, b2, tag);
    p = fresh(p);
    if (b == 0 && threadIdx.x < 64) {
        unsigned f[4];
#pragma unroll
        for (int i = 0; i < 4; ++i) asm volatile("global_load_dword %0, %1, %2 sc1" : "=v"(f[i]) : "v"(threadIdx.x * (unsigned)FL), "s"(p->ws + WS_FH + i * 64 * FL) : "memory");
        asm volatile("s_waitcnt vmcnt(0)" : "+v"(f[0]), "+v"(f[1]), "+v"(f[2]), "+v"(f[3]) :: "memory");
        if (!__all(f[0] == tag && f[1] == tag && f[2] == tag && f[3] == tag)) {
#pragma unroll 1
            for (int i = 0; i < 4; ++i) flag_wait(p->ws, WS_FH + (size_t)(i * 64 + threadIdx.x) * FL, true, tag);
        }
        if (threadIdx.x == 0) flag_set(p->ws, WS_EPOCH, tag);
    }
}

extern "C" void kernel_launch(void* const* d_in, const int* in_sizes, int n_in, void* d_out, int out_size, void* d_ws, size_t ws_size, hipStream_t stream) {
    Params p;
    p.x = (const u16*)d_in[0]; p.kk = (const u16*)d_in[1]; p.vv = (const u16*)d_in[2]; p.shk = (const u16*)d_in[3]; p.hk = (const u16*)d_in[4]; p.dvec = (const u16*)d_in[5];
    p.wg = (const u16*)d_in[7]; p.wu = (const u16*)d_in[8]; p.wd = (const u16*)d_in[9]; p.out = (u16*)d_out; p.ws = (char*)d_ws;
    constexpr int LDS_BYTES = 163840;
    (void)hipFuncSetAttribute((const void*)fused_kernel, hipFuncAttributeMaxDynamicSharedMemorySize, LDS_BYTES);
    fused_kernel<<<256, 512, LDS_BYTES, stream>>>(p);
}
```
